```python
import torch

def _softmax(x, axis=-1):
    return torch.softmax(x, dim=axis)

def _gelu(x):
    return torch.nn.functional.gelu(x, approximate='tanh')

def _silu(x):
    return torch.nn.functional.silu(x)

def _sigmoid(x):
    return torch.sigmoid(x)

def _layernorm(x, axis=-1, eps=1e-05):
    m = x.mean(dim=axis, keepdim=True)
    v = x.var(dim=axis, keepdim=True, unbiased=False)
    return (x - m) / torch.sqrt(v + eps)

def _scatter_add(tgt, idx, src, axis=0):
    return tgt.clone().index_add_(axis, idx, src)

def _segment_sum(src, seg, n):
    out = torch.zeros((n,) + src.shape[1:], dtype=src.dtype)
    return out.index_add_(0, seg, src)

def _pad(x, pw):
    flat = tuple((v for pair in reversed(pw) for v in pair))
    return torch.nn.functional.pad(x, flat)
kernel_name = 'concat_heads__ssm_conv_s4__memory_aug_recurrent_mem_tr__prompt'

def setup_inputs():

    _w = lambda a: (lambda t: t.to(torch.bfloat16) if t.is_floating_point() else t)(torch.as_tensor(a))

    def _A_setup():
        B, L, D, N = (1, 2048, 640, 128)
        A_re = -(lambda _x: torch.exp(torch.as_tensor(_x)))((lambda *s: torch.randn(*s, dtype=torch.bfloat16) * 6.4)(D, N))
        A_im = torch.pi * (lambda *s: torch.randn(*s, dtype=torch.bfloat16) * 0.05)(D, N)
        B_re = (lambda *s: torch.randn(*s, dtype=torch.bfloat16) * 0.1)(D, N)
        B_im = (lambda *s: torch.randn(*s, dtype=torch.bfloat16) * 0.1)(D, N)
        C_re = (lambda *s: torch.randn(*s, dtype=torch.bfloat16) * 0.1)(D, N)
        C_im = (lambda *s: torch.randn(*s, dtype=torch.bfloat16) * 0.1)(D, N)
        log_dt = (lambda *s: torch.randn(*s, dtype=torch.bfloat16) * 0.05)(D) * 0.5 - 4.0
        Dskip = (lambda *s: torch.randn(*s, dtype=torch.bfloat16) * 0.05)(D)
        return {'u': _w((lambda *s: torch.randn(*s, dtype=torch.bfloat16) * 0.05)(B, L, D)), 'A_re': _w(A_re), 'A_im': _w(A_im), 'B_re': _w(B_re), 'B_im': _w(B_im), 'C_re': _w(C_re), 'C_im': _w(C_im), 'log_dt': _w(log_dt), 'Dskip': _w(Dskip)}
    _A_in = _A_setup()

    def _B_setup():
        B, S, Dm = (1, 2048, 640)
        H, Dh = (32, 128)
        M, seg = (8, 32)
        seg = min(seg, S)
        S = S // seg * seg
        return {'x': _w((lambda *s: torch.randn(*s, dtype=torch.bfloat16) * 0.05)(B, S, Dm)), 'mem0': _w((lambda *s: torch.randn(*s, dtype=torch.bfloat16) * 0.4)(B, M, Dm)), 'Wq': _w((lambda *s: torch.randn(*s, dtype=torch.bfloat16) * 6.4)(Dm, H, Dh) * Dm ** (-0.5)), 'Wk': _w((lambda *s: torch.randn(*s, dtype=torch.bfloat16) * 6.4)(Dm, H, Dh) * Dm ** (-0.5)), 'Wv': _w((lambda *s: torch.randn(*s, dtype=torch.bfloat16) * 0.4)(Dm, H, Dh) * Dm ** (-0.5)), 'Wo': _w((lambda *s: torch.randn(*s, dtype=torch.bfloat16) * 0.4)(H, Dh, Dm) * (H * Dh) ** (-0.5)), 'seg_len': seg}
    _B_in = _B_setup()
    return {'A_u': _A_in['u'], 'A_A_re': _A_in['A_re'], 'A_A_im': _A_in['A_im'], 'A_B_re': _A_in['B_re'], 'A_B_im': _A_in['B_im'], 'A_C_re': _A_in['C_re'], 'A_C_im': _A_in['C_im'], 'A_log_dt': _A_in['log_dt'], 'A_Dskip': _A_in['Dskip'], 'B_x': _B_in['x'], 'B_mem0': _B_in['mem0'], 'B_Wq': _B_in['Wq'], 'B_Wk': _B_in['Wk'], 'B_Wv': _B_in['Wv'], 'B_Wo': _B_in['Wo'], 'B_seg_len': _B_in['seg_len']}

def reference(A_u, A_A_re, A_A_im, A_B_re, A_B_im, A_C_re, A_C_im, A_log_dt, A_Dskip, B_x, B_mem0, B_Wq, B_Wk, B_Wv, B_Wo, B_seg_len):

    def _A_ref(u, A_re, A_im, B_re, B_im, C_re, C_im, log_dt, Dskip):
        Bsz, L, D = u.shape
        dt = torch.exp(log_dt)[:, None]
        A = A_re + 1j * A_im
        Bc = B_re + 1j * B_im
        Cc = C_re + 1j * C_im
        logdA = dt * A
        dB = dt * Bc
        l = (lambda x, dt: x.to(dt))(torch.arange(L), A_re.dtype)
        P = torch.exp(logdA[:, :, None] * l[None, None, :])
        K = torch.einsum('dn,dnl,dn->dl', Cc, P, dB).real
        L2 = 2 * L
        Kp = _pad(K, ((0, 0), (0, L)))
        up = (lambda x, *d: x.permute(*d))(u, 0, 2, 1)
        up = _pad(up, ((0, 0), (0, 0), (0, L)))
        Kf = (lambda x, axes=(-2, -1): torch.fft.rfft2(x.float(), dim=axes))(Kp[:, None, :], axes=(-2, -1))
        Uf = (lambda x, axes=(-2, -1): torch.fft.rfft2(x.float(), dim=axes))(up[:, :, None, :], axes=(-2, -1))
        Yf = Uf * Kf[None, :, :, :]
        y = (lambda x, s, axes=(-2, -1): torch.fft.irfft2(x, s=s, dim=axes).to(torch.bfloat16))(Yf, (1, L2), axes=(-2, -1))[:, :, 0, :L]
        y = (lambda x, *d: x.permute(*d))(y, 0, 2, 1) + u * Dskip[None, None, :]
        return y

    def _B_ref(x, mem0, Wq, Wk, Wv, Wo, seg_len):
        B, S, Dm = x.shape
        H, Dh = (Wq.shape[1], Wq.shape[2])
        M = mem0.shape[1]
        scale = Dh ** (-0.5)
        n_seg = S // seg_len
        L = M + seg_len
        idx = torch.arange(L)
        mask = (idx[:, None] < M) | (idx[:, None] >= idx[None, :])
        mem = mem0
        outs = []
        for s in range(n_seg):
            xs = x[:, s * seg_len:(s + 1) * seg_len, :].clone()
            h = torch.concatenate([mem, xs], axis=1)
            q = torch.einsum('btd,dhe->bhte', h, Wq)
            k = torch.einsum('btd,dhe->bhte', h, Wk)
            v = torch.einsum('btd,dhe->bhte', h, Wv)
            scores = torch.einsum('bhqe,bhke->bhqk', q, k) * scale
            scores = torch.where(mask[None, None], scores, -1000000000.0)
            p = _softmax(scores, axis=-1)
            o = torch.einsum('bhqk,bhke->bhqe', p, v)
            o = torch.einsum('bhte,hed->btd', o, Wo)
            mem = o[:, :M, :].clone()
            outs.append(o[:, M:, :])
        return torch.concatenate(outs, axis=1)
    a = _A_ref(A_u, A_A_re, A_A_im, A_B_re, A_B_im, A_C_re, A_C_im, A_log_dt, A_Dskip)
    b = _B_ref(B_x, B_mem0, B_Wq, B_Wk, B_Wv, B_Wo, B_seg_len)
    return torch.concatenate([a, b], axis=-1)
```

```cpp
#include <hip/hip_runtime.h>
#include <hip/hip_bf16.h>

namespace {
constexpr int TUNE_DQ = 8, TUNE_DR = 12, TUNE_DZ = 10, TUNE_RETRY = 2;

#define LAS __attribute__((address_space(3)))
#define GAS __attribute__((address_space(1)))
typedef unsigned v4u __attribute__((ext_vector_type(4)));
typedef unsigned v2u __attribute__((ext_vector_type(2)));
typedef float f32x4 __attribute__((ext_vector_type(4)));
typedef float f32x2 __attribute__((ext_vector_type(2)));
typedef short s16x4 __attribute__((ext_vector_type(4)));
typedef __bf16 bf16x2 __attribute__((ext_vector_type(2)));
typedef __bf16 bf16x8 __attribute__((ext_vector_type(8)));
typedef __bf16 bf16x16 __attribute__((ext_vector_type(16)));
typedef unsigned v8u __attribute__((ext_vector_type(8)));
typedef __amdgpu_buffer_rsrc_t rsrc_t;
typedef LAS char* lds_t;

constexpr int NSEG = 64, DM = 640, NH = 32, DH = 128, SEQ = 2048;
constexpr unsigned BLK = 8192;
constexpr unsigned J_ZM = 0, J_M = 80, J_P = 104, J_END = 122;
constexpr unsigned OFF_CTL = 2 * J_END * BLK;
constexpr unsigned MEMSET_BYTES = OFF_CTL + 6 * BLK;
constexpr unsigned C_ABORT = OFF_CTL, C_NP = OFF_CTL + 128, C_TICK = OFF_CTL + 256  , C_ARR = OFF_CTL + 256 + 8 * 128, C_OVF = C_ARR + 128;
constexpr unsigned C_CNTX = OFF_CTL + 2048, C_CNTZ = C_CNTX + 64 * 128, C_CNTP = C_CNTZ + 64 * 128, C_CNTZM = C_CNTP + 64 * 128;
constexpr unsigned C_PROBE = OFF_CTL + 5 * BLK - 4096;
static_assert(C_CNTZM + 64 * 128 <= C_PROBE && C_PROBE + BLK + 4096 <= MEMSET_BYTES, "ctl");
constexpr unsigned MB = 1u << 20;
constexpr unsigned OFF_KX = 2 * MB, OFF_QX = 18 * MB, OFF_ZX = 34 * MB, OFF_ZMP = 114 * MB, OFF_PXX = 134 * MB, OFF_PXM = 138 * MB, OFF_DBG = 140 * MB, OFF_SSM = 150 * MB;
constexpr unsigned WS_USED = 200 * MB;
constexpr int AUX_SC1 = 16;
constexpr unsigned OOB = 0x80000000u;
constexpr unsigned SPIN_CAP = 1u << 15;
constexpr int LDS_BYTES = 152 * 1024;

struct Params {
    const void* x; const void* mem0; const void* Wq; const void* Wk; const void* Wv; const void* Wo;
    const void* u; const void* Are; const void* Aim; const void* Bre; const void* Bim; const void* Cre; const void* Cim; const void* logdt; const void* Dsk;
    char* ws;
    void* out;
    int rolemask; int np; int dbg; int dq; int dr; int dz; int retry; int pad0;
};

__device__ __forceinline__ rsrc_t mkrs(const void* p, unsigned bytes) { return __builtin_amdgcn_make_buffer_rsrc((void*)p, 0, bytes, 0x00020000); }
__device__ __forceinline__ v4u ld128(rsrc_t rs, unsigned off, int aux) { return aux ? __builtin_amdgcn_raw_buffer_load_b128(rs, off, 0, AUX_SC1) : __builtin_amdgcn_raw_buffer_load_b128(rs, off, 0, 0); }
__device__ __forceinline__ v4u ld128s(rsrc_t rs, unsigned off) { return __builtin_amdgcn_raw_buffer_load_b128(rs, off, 0, AUX_SC1); }
__device__ __forceinline__ v2u ld64s(rsrc_t rs, unsigned off) { return __builtin_amdgcn_raw_buffer_load_b64(rs, off, 0, AUX_SC1); }
__device__ __forceinline__ unsigned ld32s(rsrc_t rs, unsigned off) { return __builtin_amdgcn_raw_buffer_load_b32(rs, off, 0, AUX_SC1); }
__device__ __forceinline__ v4u ld128so(rsrc_t rs, unsigned voff, unsigned soff) { return __builtin_amdgcn_raw_buffer_load_b128(rs, voff, soff, AUX_SC1); }
__device__ __forceinline__ v2u ld64so(rsrc_t rs, unsigned voff, unsigned soff) { return __builtin_amdgcn_raw_buffer_load_b64(rs, voff, soff, AUX_SC1); }
__device__ __forceinline__ void st128so(rsrc_t rs, unsigned voff, unsigned soff, v4u v) { __builtin_amdgcn_raw_buffer_store_b128(v, rs, voff, soff, AUX_SC1); }
__device__ __forceinline__ void st64so(rsrc_t rs, unsigned voff, unsigned soff, v2u v) { __builtin_amdgcn_raw_buffer_store_b64(v, rs, voff, soff, AUX_SC1); }
__device__ __forceinline__ unsigned umax3(unsigned a, unsigned b, unsigned c) { return max(max(a, b), c); }
__device__ __forceinline__ unsigned umax4(unsigned m, v4u v) { return umax3(umax3(m, v.x, v.y), v.z, v.w); }
__device__ __forceinline__ void st128s(rsrc_t rs, unsigned off, v4u v) { __builtin_amdgcn_raw_buffer_store_b128(v, rs, off, 0, AUX_SC1); }
__device__ __forceinline__ void st64s(rsrc_t rs, unsigned off, v2u v) { __builtin_amdgcn_raw_buffer_store_b64(v, rs, off, 0, AUX_SC1); }
__device__ __forceinline__ void st32s(rsrc_t rs, unsigned off, unsigned v) { __builtin_amdgcn_raw_buffer_store_b32(v, rs, off, 0, AUX_SC1); }
__device__ __forceinline__ v4u mksrd(const void* p, unsigned bytes) { const unsigned long long a = (unsigned long long)p; return v4u{(unsigned)a, (unsigned)(a >> 32) & 0xffffu, bytes, 0x00020000u}; }
__device__ __forceinline__ void dma16(v4u srd, lds_t dst, unsigned voff) {
    unsigned keep; const unsigned la = (unsigned)(unsigned long long)dst;
    asm volatile("s_mov_b32 %0, m0\n\ts_mov_b32 m0, %3\n\ts_nop 0\n\tbuffer_load_dwordx4 %1, %2, 0 offen lds\n\ts_mov_b32 m0, %0" : "=&s"(keep) : "v"(voff), "s"(srd), "s"(la) : "memory");
}
__device__ __forceinline__ void dma16s(v4u srd, lds_t dst, unsigned voff) {
    unsigned keep; const unsigned la = (unsigned)(unsigned long long)dst;
    asm volatile("s_mov_b32 %0, m0\n\ts_mov_b32 m0, %3\n\ts_nop 0\n\tbuffer_load_dwordx4 %1, %2, 0 offen sc1 lds\n\ts_mov_b32 m0, %0" : "=&s"(keep) : "v"(voff), "s"(srd), "s"(la) : "memory");
}
__device__ __forceinline__ void bar() { asm volatile("s_waitcnt lgkmcnt(0)\n\ts_barrier" ::: "memory"); }
__device__ __forceinline__ void drain() { asm volatile("s_waitcnt vmcnt(0)" ::: "memory"); }
__device__ __forceinline__ unsigned pk(float a, float b) { f32x2 v = {a, b}; return __builtin_bit_cast(unsigned, __builtin_convertvector(v, bf16x2)); }
__device__ __forceinline__ unsigned safe(unsigned x) { return x < 0xFFFFFFFEu ? x : 0xFFFFFFFEu; }
__device__ __forceinline__ bool valid4(v4u v) { return (v.x != 0xFFFFFFFFu) & (v.y != 0xFFFFFFFFu) & (v.z != 0xFFFFFFFFu) & (v.w != 0xFFFFFFFFu); }
__device__ __forceinline__ bool valid2(v2u v) { return (v.x != 0xFFFFFFFFu) & (v.y != 0xFFFFFFFFu); }
__device__ __forceinline__ v2u trd(lds_t p) { return __builtin_bit_cast(v2u, __builtin_amdgcn_ds_read_tr16_b64_v4i16((LAS s16x4*)p)); }
__device__ __forceinline__ f32x4 smf(v4u a, v8u b, f32x4 c, int idx) { return __builtin_amdgcn_smfmac_f32_16x16x64_bf16(__builtin_bit_cast(bf16x8, a), __builtin_bit_cast(bf16x16, b), c, idx, 0, 0); }
__device__ __forceinline__ f32x4 mf32(v4u a, v4u b, f32x4 c) { return __builtin_amdgcn_mfma_f32_16x16x32_bf16(__builtin_bit_cast(bf16x8, a), __builtin_bit_cast(bf16x8, b), c, 0, 0, 0); }
__device__ __forceinline__ f32x4 mf16(v2u a, v2u b, f32x4 c) { return __builtin_amdgcn_mfma_f32_16x16x16bf16_1k(__builtin_bit_cast(s16x4, a), __builtin_bit_cast(s16x4, b), c, 0, 0, 0); }
template <int CTRL> __device__ __forceinline__ float dppf(float v) { return __builtin_bit_cast(float, __builtin_amdgcn_update_dpp(0, __builtin_bit_cast(int, v), CTRL, 0xf, 0xf, true)); }
template <int CTRL> __device__ __forceinline__ unsigned dppu(unsigned v) { return (unsigned)__builtin_amdgcn_update_dpp(0, (int)v, CTRL, 0xf, 0xf, true); }
__device__ __forceinline__ float asf(unsigned u) { return __builtin_bit_cast(float, u); }
__device__ __forceinline__ unsigned asu(float f) { return __builtin_bit_cast(unsigned, f); }
__device__ __forceinline__ float swap32_sum(float x) { auto r = __builtin_amdgcn_permlane32_swap(asu(x), asu(x), false, false); const unsigned a = r[0], b = r[1]; return asf(a) + asf(b); }
__device__ __forceinline__ float swap32_max(float x) { auto r = __builtin_amdgcn_permlane32_swap(asu(x), asu(x), false, false); const unsigned a = r[0], b = r[1]; return fmaxf(asf(a), asf(b)); }
__device__ __forceinline__ float swap16_sum(float x) { auto r = __builtin_amdgcn_permlane16_swap(asu(x), asu(x), false, false); const unsigned a = r[0], b = r[1]; return asf(a) + asf(b); }
__device__ __forceinline__ float swap16_max(float x) { auto r = __builtin_amdgcn_permlane16_swap(asu(x), asu(x), false, false); const unsigned a = r[0], b = r[1]; return fmaxf(asf(a), asf(b)); }
__device__ __forceinline__ float fold2(float a, float b) { auto r = __builtin_amdgcn_permlane32_swap(asu(a), asu(b), false, false); const unsigned x = r[0], y = r[1]; return asf(x) + asf(y); }
__device__ __forceinline__ float vmax(float a, float b) { float r; asm("v_max_f32 %0, %1, %2" : "=v"(r) : "v"(a), "v"(b)); return r; }
__device__ __forceinline__ float wave_max(float x) {
    float y;
    asm("s_nop 1\n\tv_max_f32_dpp %0, %1, %1 row_ror:8 row_mask:0xf bank_mask:0xf\n\ts_nop 1\n\tv_max_f32_dpp %0, %0, %0 row_ror:4 row_mask:0xf bank_mask:0xf\n\ts_nop 1\n\t"
        "v_max_f32_dpp %0, %0, %0 row_ror:2 row_mask:0xf bank_mask:0xf\n\ts_nop 1\n\tv_max_f32_dpp %0, %0, %0 row_ror:1 row_mask:0xf bank_mask:0xf\n\ts_nop 1" : "=&v"(y) : "v"(x));
    { auto r = __builtin_amdgcn_permlane16_swap(asu(y), asu(y), false, false); const unsigned a = r[0], b = r[1]; y = vmax(asf(a), asf(b)); }
    { auto r = __builtin_amdgcn_permlane32_swap(asu(y), asu(y), false, false); const unsigned a = r[0], b = r[1]; y = vmax(asf(a), asf(b)); }
    return y;
}
__device__ __forceinline__ float wave_sum(float x) { x += dppf<0x128>(x); x += dppf<0x124>(x); x += dppf<0x122>(x); x += dppf<0x121>(x); x = swap16_sum(x); return swap32_sum(x); }

struct Ctx {
    rsrc_t ws; lds_t L; int lane, w; int np;
};
__device__ __forceinline__ unsigned nblk(int j, int np) { return (unsigned)(2 * j + np) * BLK; }
__device__ __forceinline__ unsigned zm_ring(int slot, int c, int np) { return nblk(J_ZM + slot * 40 + c, np); }
__device__ __forceinline__ unsigned m_base(int slot, int copy, int np) { return nblk(J_M + 2 * (slot * 4 + copy), np); }
__device__ __forceinline__ unsigned p_base(int slot, int copy, int np) { return nblk(J_P + 3 * (slot * 3 + copy), np); }
__device__ __forceinline__ unsigned can_off(int kind, int cls, int np) { return nblk(J_P + 2, np) + 4096 + (kind * 4 + cls) * 128; }
__host__ __device__ constexpr unsigned fragoff(int f) { return (unsigned)((f >> 3) * 16384 + (f & 7) * 1024); }

__device__ __forceinline__ bool aborted(rsrc_t ws) { return __builtin_amdgcn_readfirstlane(ld32s(ws, C_ABORT)) != 0xFFFFFFFFu; }
__device__ __forceinline__ void set_abort(rsrc_t ws, unsigned code) { st32s(ws, C_ABORT, code); }
__device__ __forceinline__ bool spin_more(rsrc_t ws, unsigned& spins, unsigned code) {
    ++spins;
    if (spins > SPIN_CAP) { set_abort(ws, code); return false; }
    if ((spins & 63u) == 0u && aborted(ws)) return false;
    return true;
}
__device__ __forceinline__ bool wait_cnt(rsrc_t ws, unsigned off, unsigned n, unsigned code) {
    unsigned spins = 0;
    for (;;) {
        asm volatile("" ::: "memory");
        unsigned v = __builtin_amdgcn_readfirstlane(ld32s(ws, off));
        if (v + 1u >= n) return true;
        __builtin_amdgcn_s_sleep(2);
        if (!spin_more(ws, spins, code)) return false;
    }
}
#define DEVDBG 0
#define QCAN 1
#define RCAN 7
#define VCAN 1
#define STAMPLITE 0
#define STAMPS 0
constexpr int STAMP_OFF = 148 * 1024;
struct TS {
    unsigned acc[12]; unsigned last; unsigned r0, c0;
    __device__ __forceinline__ static unsigned now() { unsigned long long t; asm volatile("s_memtime %0\n\ts_waitcnt lgkmcnt(0)" : "=s"(t) :: "memory"); return (unsigned)t; }
    __device__ __forceinline__ void init() { if (STAMPLITE) r0 = (unsigned)__builtin_amdgcn_s_memrealtime(); if (STAMPS) { for (int i = 0; i < 12; ++i) acc[i] = 0; r0 = (unsigned)__builtin_amdgcn_s_memrealtime(); c0 = now(); last = c0; } }
    __device__ __forceinline__ void mark(int i, bool on = true) { if (STAMPS) { __builtin_amdgcn_sched_barrier(0); const unsigned t = now(); if (on) acc[i] += t - last; last = t; __builtin_amdgcn_sched_barrier(0); } }
    __device__ __forceinline__ void dump(const Params& P, int who, unsigned extra) {
        if (STAMPLITE) { const unsigned r1 = (unsigned)__builtin_amdgcn_s_memrealtime(); if ((threadIdx.x & 63) == 0) { unsigned* o = (unsigned*)(P.ws + OFF_DBG + 8 * MB) + (who * 8 + (threadIdx.x >> 6)) * 16; o[12] = r1 - r0; o[13] = 24 * (r1 - r0); o[14] = extra; } }
        if (STAMPS) { const unsigned r1 = (unsigned)__builtin_amdgcn_s_memrealtime(), c1 = now();
            if ((threadIdx.x & 63) == 0) { unsigned* o = (unsigned*)(P.ws + OFF_DBG + 8 * MB) + (who * 8 + (threadIdx.x >> 6)) * 16; for (int i = 0; i < 12; ++i) o[i] = acc[i]; o[12] = r1 - r0; o[13] = c1 - c0; o[14] = extra; } }
    }
};
__device__ __forceinline__ void cnt_add(char* wsbase, unsigned off) { __hip_atomic_fetch_add((GAS unsigned*)(wsbase + off), 1u, __ATOMIC_RELAXED, __HIP_MEMORY_SCOPE_AGENT); }

template <int SIG> __device__ __forceinline__ int sigma(int row) {
    if (SIG == 0) return (row & 1) | (((row >> 2) & 1) << 1) | (((row >> 3) & 1) << 2);
    if (SIG == 1) return (row & 3) | (((row >> 3) & 1) << 2);
    return row & 7;
}
template <int SIG> __device__ __forceinline__ void stage_rows256(v4u rw, lds_t dst, int row0, int h, int w, int lane) {
#pragma unroll
    for (int i = 0; i < 2; ++i) {
        const int ins = 2 * w + i, r = 4 * ins + (lane >> 4), s = lane & 15;
        const int col = (((s >> 1) ^ sigma<SIG>(r)) << 1) | (s & 1);
        dma16(rw, dst + ins * 1024, (unsigned)(row0 + r) * 8192u + (unsigned)h * 256u + (unsigned)col * 16u);
    }
}
template <int SIG> __device__ __forceinline__ void stage_rows1280(v4u rw, lds_t dst, int e0, int h, int w, int lane) {
#pragma unroll
    for (int i = 0; i < 10; ++i) {
        const int ins = 10 * w + i, S = ins * 64 + lane, r = S / 80, s = S % 80, s16 = s & 15;
        const int col = (s & ~15) | ((((s16 >> 1) ^ sigma<SIG>(r)) << 1) | (s16 & 1));
        dma16(rw, dst + ins * 1024, (unsigned)(h * 128 + e0 + r) * 1280u + (unsigned)col * 16u);
    }
}
__device__ __forceinline__ v8u rd_sparseB(lds_t base, int stride, int pair, int lane) {
    const int g = lane >> 4, q = (lane >> 2) & 3, p = lane & 3;
    v8u o;
#pragma unroll
    for (int b = 0; b < 2; ++b)
#pragma unroll
        for (int rho = 0; rho < 2; ++rho) {
            const int row = 32 * b + 16 * (g >> 1) + 4 * (g & 1) + 2 * rho + (q & 1) + 8 * (q >> 1);
            const v2u t = trd(base + row * stride + (pair >> 3) * 256 + (((pair & 7) ^ sigma<0>(row)) * 32) + 8 * p);
            o[4 * b + 2 * rho] = t.x; o[4 * b + 2 * rho + 1] = t.y;
        }
    return o;
}
__device__ __forceinline__ v4u rd_denseNat(lds_t base, int stride, int row0, int pair, int lane) {
    const int g = lane >> 4, q = (lane >> 2) & 3, p = lane & 3;
    v4u o;
#pragma unroll
    for (int rho = 0; rho < 2; ++rho) {
        const int row = row0 + 8 * g + 4 * rho + q;
        const v2u t = trd(base + row * stride + (pair >> 3) * 256 + (((pair & 7) ^ sigma<1>(row)) * 32) + 8 * p);
        o[2 * rho] = t.x; o[2 * rho + 1] = t.y;
    }
    return o;
}
__device__ __forceinline__ v4u rd_densePerm(lds_t base, int stride, int row0, int pair, int lane) {
    const int g = lane >> 4, q = (lane >> 2) & 3, p = lane & 3;
    v4u o;
#pragma unroll
    for (int rho = 0; rho < 2; ++rho) {
        const int row = row0 + 16 * rho + 4 * g + q;
        const v2u t = trd(base + row * stride + (pair >> 3) * 256 + (((pair & 7) ^ sigma<2>(row)) * 32) + 8 * p);
        o[2 * rho] = t.x; o[2 * rho + 1] = t.y;
    }
    return o;
}

__device__ __forceinline__ unsigned rtime() { return (unsigned)__builtin_amdgcn_s_memrealtime(); }
__device__ __forceinline__ bool canary_wait(rsrc_t ws, unsigned off, int want, int dly, unsigned code) {
    unsigned v0 = ld32s(ws, off); __builtin_amdgcn_s_sleep(1);
    unsigned v1 = ld32s(ws, off); __builtin_amdgcn_s_sleep(1);
    unsigned v2 = ld32s(ws, off); __builtin_amdgcn_s_sleep(1);
    unsigned v3 = ld32s(ws, off);
    unsigned spins = 0;
    for (;;) {
        if ((int)__builtin_amdgcn_readfirstlane(v0) >= want) break;
        asm volatile("" ::: "memory"); v0 = ld32s(ws, off);
        if ((int)__builtin_amdgcn_readfirstlane(v1) >= want) break;
        asm volatile("" ::: "memory"); v1 = ld32s(ws, off);
        if ((int)__builtin_amdgcn_readfirstlane(v2) >= want) break;
        asm volatile("" ::: "memory"); v2 = ld32s(ws, off);
        if ((int)__builtin_amdgcn_readfirstlane(v3) >= want) break;
        asm volatile("" ::: "memory"); v3 = ld32s(ws, off);
        if (!spin_more(ws, spins, code)) return false;
    }
    for (int z = 0; z < dly; ++z) __builtin_amdgcn_s_sleep(1);
    return true;
}

__device__ __forceinline__ bool gather_m(const Params& P, rsrc_t ws, lds_t Mb, int s, int cls, int w, int lane, int np, unsigned& nfail) {
    if (s == 0) {
        const unsigned short* m0 = (const unsigned short*)P.mem0;
#pragma unroll
        for (int it = 0; it < 2; ++it) {
            const int d = threadIdx.x + 512 * it;
            if (d < DM) {
                unsigned v[8];
#pragma unroll
                for (int i = 0; i < 8; ++i) v[i] = m0[i * DM + d];
                *(LAS v4u*)(Mb + (d >> 4) * 256 + ((d & 15) ^ (4 * ((d >> 4) & 1))) * 16) = v4u{v[0] | (v[1] << 16), v[2] | (v[3] << 16), v[4] | (v[5] << 16), v[6] | (v[7] << 16)};
            }
        }
        return true;
    }
    const bool two = w >= 6;
    const int f2 = w + 2;
    const unsigned sb = m_base(s % 3, cls, np);
    const unsigned oa = w * 1024 + lane * 16, ob = two ? 16384 + (w - 6) * 1024 + lane * 16 : OOB;
    if (w == 5) { if (!canary_wait(ws, can_off(0, cls, np), s, P.dq, 0x180 + s)) return false; }
    asm volatile("s_barrier" ::: "memory");
    v4u a = ld128so(ws, oa, sb), b = ld128so(ws, ob, sb);
    if (!__all(umax4(umax4(0u, a), b) != 0xFFFFFFFFu)) {
        unsigned spins = 0;
        ++nfail;
        for (;;) {
            asm volatile("" ::: "memory");
            for (int z = 0; z < P.retry; ++z) __builtin_amdgcn_s_sleep(1);
            if (!valid4(a)) a = ld128so(ws, oa, sb);
            if (!valid4(b)) b = ld128so(ws, ob, sb);
            if (__all(valid4(a) && valid4(b))) break;
            if (!spin_more(ws, spins, 0x100 + s)) return false;
        }
    }
    *(LAS v4u*)(Mb + w * 1024 + lane * 16) = a;
    if (two) *(LAS v4u*)(Mb + f2 * 1024 + lane * 16) = b;
    return true;
}
__device__ __forceinline__ unsigned afrag_off(int lane) { const int g = lane >> 4, q = (lane >> 2) & 3, p = lane & 3; return g * 256 + ((8 * (p >> 1) + q) ^ (4 * (g & 1))) * 16 + (p & 1) * 8; }
__device__ __forceinline__ v4u rd_afrag(lds_t base, unsigned a0, int k) { const v2u x = trd(base + k * 1024 + a0), y = trd(base + k * 1024 + (a0 ^ 64)); return v4u{x.x, x.y, y.x, y.y}; }

__device__ __forceinline__ void role_qk(const Params& P, lds_t L, int h, int np) {
    const int tid = threadIdx.x, lane = tid & 63, w = __builtin_amdgcn_readfirstlane(tid >> 6);
    const rsrc_t ws = mkrs(P.ws, WS_USED);
    const v4u rq = mksrd(P.Wq, DM * 4096 * 2), rk = mksrd(P.Wk, DM * 4096 * 2), wsd = mksrd(P.ws, WS_USED);
    v8u Wq[10], Wk[10];
    {
        auto stage = [&](int st) {
            lds_t b = L + (st & 1) * 65536;
            stage_rows256<0>(rq, b, 128 * st, h, w, lane); stage_rows256<0>(rq, b + 16384, 128 * st + 64, h, w, lane);
            stage_rows256<0>(rk, b + 32768, 128 * st, h, w, lane); stage_rows256<0>(rk, b + 49152, 128 * st + 64, h, w, lane);
        };
        stage(0); stage(1);
#pragma unroll
        for (int st = 0; st < 5; ++st) {
            if (st < 4) asm volatile("s_waitcnt vmcnt(8)" ::: "memory"); else drain();
            bar();
            lds_t b = L + (st & 1) * 65536;
            Wq[2 * st] = rd_sparseB(b, 256, w, lane); Wq[2 * st + 1] = rd_sparseB(b + 16384, 256, w, lane);
            Wk[2 * st] = rd_sparseB(b + 32768, 256, w, lane); Wk[2 * st + 1] = rd_sparseB(b + 49152, 256, w, lane);
            bar();
            if (st + 2 < 5) stage(st + 2);
        }
    }
    lds_t Mb = L;
    lds_t QT = L + 10240, KT = QT + 2048;
    lds_t SP = L + 14336;
    lds_t XB = L + 28672;
    const unsigned a0 = afrag_off(lane);
    const int sidx = ((lane >> 3) & 1) ? 0xEEEE : 0x4444;
    lds_t spx = SP + w * 1664 + (lane & 7) * 208 + 64 + ((lane >> 3) & 1) * 64 + (lane >> 4) * 16;
    lds_t spm = (lane & 15) < 8 ? SP + w * 1664 + (lane & 7) * 208 + (lane >> 4) * 16 : L + 61440 + (lane & 7) * 16 + (lane >> 4) * 128;
    int scol;
    { const int pi = lane >> 3, gl = pi >> 1, hf = pi & 1, j = lane & 7, q = j >> 1, r = j & 1;
      scol = lane < 32 ? 16 + 16 * (q & 1) + 8 * gl + 4 * (q >> 1) + 2 * hf + r : (lane < 40 ? lane - 32 : 0); }
    lds_t spr = SP + w * 208 + scol * 4;
    lds_t PB = L + 27648;
    lds_t pbw = (lane & 7) == 0 && lane <= 32 ? PB + (lane >> 3) * 128 + w * 16 : L + 61440 + lane * 16;
    const unsigned pofs = (w >= 3 || lane >= 40) ? OOB : (lane < 32 ? ((h >> 1) >> 3) * 16384 + ((h >> 1) & 7) * 1024 + (h & 1) * 512 + lane * 16 : 2 * 16384 + (h >> 3) * 1024 + (h & 7) * 128 + (lane - 32) * 16);
    const unsigned pb0 = p_base(0, w < 3 ? w : 0, np), pb1 = p_base(1, w < 3 ? w : 0, np);
    const unsigned xo = (unsigned)h * 8192u + w * 1024 + lane * 16;
    auto dma_x = [&](int s) {
        lds_t xb = XB + (s & 1) * 16384;
        dma16s(wsd, xb + w * 1024, OFF_KX + (unsigned)s * (32u * 8192u) + xo);
        dma16s(wsd, xb + 8192 + w * 1024, OFF_QX + (unsigned)s * (32u * 8192u) + xo);
    };
    if (!wait_cnt(ws, C_CNTX, 32, 0x200)) return;
    dma_x(0);
    const v4u ones = {0xFFFFFFFFu, 0xFFFFFFFFu, 0xFFFFFFFFu, 0xFFFFFFFFu};
    unsigned nfail = 0;
    const unsigned cano = (h == 0 && w == 0 && lane >= 1 && lane < 4) ? can_off(1, lane, np) : OOB;
    TS ts; ts.init();
    for (int s = 0; s < NSEG; ++s) {
        const bool on = s >= 8 && s < 60;
        if (!gather_m(P, ws, Mb, s, 0, w, lane, np, nfail)) return;
        ts.mark(0, on);
        drain();
        bar();
        if (STAMPS > 2 && w == 0 && s >= 30 && s < 38) { const unsigned t_ = rtime(); if (lane == 0) ((unsigned*)(P.ws + OFF_DBG + 9 * MB))[(h * 8 + (s - 30)) * 4 + 0] = t_; }
        ts.mark(1, on);
        const bool last = s == NSEG - 1;
        lds_t xb = XB + (s & 1) * 16384;
        v4u af[5];
#pragma unroll
        for (int k = 0; k < 5; ++k) af[k] = rd_afrag(Mb, a0, k);
        const v2u kx0 = *(LAS v2u*)(xb + ((w >> 1) * 2 + 0) * 1024 + lane * 16 + 8 * (w & 1));
        const v2u kx1 = *(LAS v2u*)(xb + ((w >> 1) * 2 + 1) * 1024 + lane * 16 + 8 * (w & 1));
        st128so(ws, (s >= 1 && !last) ? pofs : OOB, (s & 1) ? pb0 : pb1, ones);
        const unsigned cx = ld32s(ws, last ? OOB : C_CNTX + (s + 1) * 128);
        __builtin_amdgcn_sched_barrier(0);
        f32x4 aq = {0.f, 0.f, 0.f, 0.f}, ak = {0.f, 0.f, 0.f, 0.f};
#pragma unroll
        for (int k = 0; k < 5; ++k) { aq = smf(af[k], Wq[k], aq, sidx); ak = smf(af[k], Wk[k], ak, sidx); af[k] = rd_afrag(Mb, a0, 5 + k); __builtin_amdgcn_sched_barrier(0); }
#pragma unroll
        for (int k = 0; k < 5; ++k) { aq = smf(af[k], Wq[5 + k], aq, sidx); ak = smf(af[k], Wk[5 + k], ak, sidx); }
        const float v0 = fold2(aq[0], ak[0]), v1 = fold2(aq[1], ak[1]), v2 = fold2(aq[2], ak[2]), v3 = fold2(aq[3], ak[3]);
        *(LAS v2u*)(QT + (lane >> 5) * 2048 + (16 * w + (lane & 15)) * 16 + ((lane >> 4) & 1) * 8) = v2u{pk(v0, v1), pk(v2, v3)};
        if (!last) {
            asm volatile("" ::: "memory");
            const int tro = (16 * w + 4 * (lane >> 4) + ((lane >> 2) & 3)) * 16 + (lane & 1) * 8;
            const v2u qf = trd(QT + tro), kf = trd(KT + tro);
            const f32x4 z = {0.f, 0.f, 0.f, 0.f};
            f32x4 s0 = mf16(kx0, qf, z), s1 = mf16(kx1, qf, z), sm = mf16(kf, qf, z);
#pragma unroll
            for (int r = 0; r < 4; ++r) s0[r] = asf((unsigned)__builtin_amdgcn_update_dpp((int)asu(s0[r]), (int)asu(s1[r]), 0x118, 0xf, 0xc, false));
            *(LAS f32x4*)spx = s0; *(LAS f32x4*)spm = sm;
        }
        if (QCAN == 0) st32s(ws, cano, (unsigned)(s + 1));
        ts.mark(2, on);
        bar();
        if (QCAN == 1) st32s(ws, cano, (unsigned)(s + 1));
        ts.mark(3, on);
        if ((DEVDBG && (P.dbg & 1)) && s == 0) { ((v2u*)(P.ws + OFF_DBG + 6 * MB + h * 4096))[tid] = *(LAS v2u*)(QT + tid * 8); }
        if (!last) {
            float pt[8];
#pragma unroll
            for (int ww = 0; ww < 8; ++ww) pt[ww] = *(LAS float*)(spr + ww * 1664);
            float sv = ((pt[0] + pt[1]) + (pt[2] + pt[3])) + ((pt[4] + pt[5]) + (pt[6] + pt[7]));
            if (lane >= 40) sv = -INFINITY;
            if (QCAN == 2) { asm volatile("" : "+v"(sv)); st32s(ws, cano, (unsigned)(s + 1)); }
            const float mx = wave_max(sv);
            const float e = __builtin_amdgcn_exp2f((sv - mx) * (0.08838834764831845f * 1.4426950408889634f));
            const float sum = wave_sum(e);
            const float p = e * __builtin_amdgcn_rcpf(sum);
            const unsigned d0 = safe(pk(p, dppf<0x101>(p)));
            const unsigned d1 = dppu<0x102>(d0), d2 = dppu<0x104>(d0), d3 = dppu<0x106>(d0);
            *(LAS v4u*)pbw = v4u{d0, d1, d2, d3};
            drain();
            bar();
            if (w < 3) { const v4u pv = *(LAS v4u*)(PB + (lane < 40 ? lane : 0) * 16); st128so(ws, pofs, (s & 1) ? pb1 : pb0, pv); }
            if (STAMPS > 2 && w == 0 && s >= 30 && s < 38) { const unsigned t_ = rtime(); if (lane == 0) ((unsigned*)(P.ws + OFF_DBG + 9 * MB))[(h * 8 + (s - 30)) * 4 + 1] = t_; }
            ts.mark(4, on);
            if ((DEVDBG && (P.dbg & 1)) && lane < 40) ((float*)(P.ws + OFF_DBG + 2 * MB))[((s * 32 + h) * 8 + w) * 48 + scol] = p;
        }
        if (s >= 1 && tid == 0) cnt_add(P.ws, C_CNTP + (s - 1) * 128);
        if (!last) {
            if (__builtin_amdgcn_readfirstlane(cx) + 1u < 32u) { if (!wait_cnt(ws, C_CNTX + (s + 1) * 128, 32, 0x200 + s)) return; }
            dma_x(s + 1);
        }
        if (w == 2 || w == 3) {
            const int tq = w - 2;
            f32x4 tm = {0.f, 0.f, 0.f, 0.f}, t0 = tm, t1 = tm;
#pragma unroll
            for (int k = 0; k < 4; ++k) {
                const v4u qf = *(LAS v4u*)(xb + 8192 + (k * 2 + tq) * 1024 + lane * 16);
                const v4u k0 = *(LAS v4u*)(xb + (k * 2 + 0) * 1024 + lane * 16), k1 = *(LAS v4u*)(xb + (k * 2 + 1) * 1024 + lane * 16);
                const int tro = (32 * k + 4 * (lane >> 4) + ((lane >> 2) & 3)) * 16 + (lane & 1) * 8;
                const v2u ma = trd(KT + tro), mb = trd(KT + tro + 256);
                tm = mf32(v4u{ma.x, ma.y, mb.x, mb.y}, qf, tm); t0 = mf32(k0, qf, t0); t1 = mf32(k1, qf, t1);
            }
            const int ii = 16 * tq + (lane & 15), g = lane >> 4;
            float sv[12];
#pragma unroll
            for (int r = 0; r < 4; ++r) {
                sv[r] = g < 2 ? tm[r] : -INFINITY;
                sv[4 + r] = (4 * g + r <= ii) ? t0[r] : -INFINITY;
                sv[8 + r] = (16 + 4 * g + r <= ii) ? t1[r] : -INFINITY;
            }
            float mx = sv[0];
#pragma unroll
            for (int r = 1; r < 12; ++r) mx = fmaxf(mx, sv[r]);
            mx = swap32_max(swap16_max(mx));
            float sum = 0.f;
#pragma unroll
            for (int r = 0; r < 12; ++r) { sv[r] = __builtin_amdgcn_exp2f((sv[r] - mx) * (0.08838834764831845f * 1.4426950408889634f)); sum += sv[r]; }
            sum = swap32_sum(swap16_sum(sum));
            const float inv = __builtin_amdgcn_rcpf(sum);
            st128s(ws, OFF_PXX + (unsigned)((s * 32 + h) * 2 + tq) * 1024u + lane * 16, v4u{pk(sv[4] * inv, sv[5] * inv), pk(sv[6] * inv, sv[7] * inv), pk(sv[8] * inv, sv[9] * inv), pk(sv[10] * inv, sv[11] * inv)});
            st64s(ws, g < 2 ? OFF_PXM + (unsigned)(s * 32 + h) * 512u + ii * 16 + g * 8 : OOB, v2u{pk(sv[0] * inv, sv[1] * inv), pk(sv[2] * inv, sv[3] * inv)});
        }
        ts.mark(5, on);
    }
    drain(); bar();
    if (tid == 0) cnt_add(P.ws, C_CNTP + (NSEG - 1) * 128);
    ts.dump(P, h, nfail);
}

__device__ __forceinline__ void role_vo(const Params& P, lds_t L, int h, int cls, int np) {
    const int tid = threadIdx.x, lane = tid & 63, w = __builtin_amdgcn_readfirstlane(tid >> 6);
    const rsrc_t ws = mkrs(P.ws, WS_USED);
    const v4u rv = mksrd(P.Wv, DM * 4096 * 2), ro = mksrd(P.Wo, 4096 * DM * 2);
    v8u Wv[10], Wo[10];
    {
        auto stage = [&](int st) { lds_t b = L + (st & 1) * 32768; stage_rows256<0>(rv, b, 128 * st, h, w, lane); stage_rows256<0>(rv, b + 16384, 128 * st + 64, h, w, lane); };
        stage(0); stage(1);
        stage_rows1280<0>(ro, L + 65536, 0, h, w, lane);
#pragma unroll
        for (int st = 0; st < 5; ++st) {
            drain();
            bar();
            lds_t b = L + (st & 1) * 32768;
            Wv[2 * st] = rd_sparseB(b, 256, w, lane); Wv[2 * st + 1] = rd_sparseB(b + 16384, 256, w, lane);
            if (st == 0) {
#pragma unroll
                for (int t = 0; t < 5; ++t) Wo[2 * t] = rd_sparseB(L + 65536, 1280, 5 * w + t, lane);
            }
            if (st == 1) {
#pragma unroll
                for (int t = 0; t < 5; ++t) Wo[2 * t + 1] = rd_sparseB(L + 65536, 1280, 5 * w + t, lane);
            }
            bar();
            if (st + 2 < 5) stage(st + 2);
            if (st == 0) stage_rows1280<0>(ro, L + 65536, 64, h, w, lane);
        }
    }
    lds_t Mb = L, VT = L + 10240;
    const unsigned a0 = afrag_off(lane);
    const int sidx = ((lane >> 3) & 1) ? 0xEEEE : 0x4444;
    const int tile4 = ((lane >> 4) & 1) * 2 + (lane >> 5);
    const unsigned zo4 = (unsigned)h * 256 + (lane & 15) * 16, zo1 = lane < 32 ? (unsigned)h * 256 + (lane & 15) * 16 + (lane >> 4) * 8 : OOB;
    const unsigned r4 = (5 * w + tile4) * 16384 + zo4, r1 = lane < 32 ? (5 * w + 4) * 16384 + zo1 : OOB;
    const unsigned q4 = (5 * w + tile4) * 8192 + zo4, q1 = lane < 32 ? (5 * w + 4) * 8192 + zo1 : OOB;
    const unsigned zb0 = zm_ring(0, 0, np), zb1 = zm_ring(1, 0, np);
    lds_t vtw = VT + (16 * w + ((lane & 15) ^ (4 * (w & 1)))) * 16 + ((lane >> 4) & 1) * 8;
    const v4u ones = {0xFFFFFFFFu, 0xFFFFFFFFu, 0xFFFFFFFFu, 0xFFFFFFFFu};
    unsigned nfail = 0;
    const unsigned cano = (h == 0 && w == 0 && lane >= 1 && lane < 4) ? can_off(2, lane, np) : OOB;
    TS ts; ts.init();
    for (int s = 0; s < NSEG; ++s) {
        const bool on = s >= 8 && s < 60;
        if (!gather_m(P, ws, Mb, s, cls, w, lane, np, nfail)) return;
        ts.mark(0, on);
        drain();
        bar();
        ts.mark(1, on);
        const bool last = s == NSEG - 1;
        v4u af[10];
#pragma unroll
        for (int k = 0; k < 10; ++k) af[k] = rd_afrag(Mb, a0, k);
        __builtin_amdgcn_sched_barrier(0);
        f32x4 av = {0.f, 0.f, 0.f, 0.f}, av2 = av;
#pragma unroll
        for (int k = 0; k < 10; k += 2) { av = smf(af[k], Wv[k], av, sidx); av2 = smf(af[k + 1], Wv[k + 1], av2, sidx); }
        av += av2;
        const float v0 = swap32_sum(av[0]), v1 = swap32_sum(av[1]), v2 = swap32_sum(av[2]), v3 = swap32_sum(av[3]);
        *(LAS v2u*)vtw = v2u{pk(v0, v1), pk(v2, v3)};
        if (VCAN == 0) st32s(ws, last ? OOB : cano, (unsigned)(s + 1));
        ts.mark(2, on);
        bar();
        if (VCAN == 1) st32s(ws, last ? OOB : cano, (unsigned)(s + 1));
        ts.mark(3, on);
        const v4u A0 = rd_afrag(VT, a0, 0), A1 = rd_afrag(VT, a0, 1);
        f32x4 az[5];
#pragma unroll
        for (int t = 0; t < 5; ++t) { az[t] = f32x4{0.f, 0.f, 0.f, 0.f}; az[t] = smf(A0, Wo[2 * t], az[t], sidx); }
#pragma unroll
        for (int t = 0; t < 5; ++t) az[t] = smf(A1, Wo[2 * t + 1], az[t], sidx);
        const unsigned a1 = safe(pk(fold2(az[0][0], az[1][0]), fold2(az[0][1], az[1][1]))), b1 = safe(pk(fold2(az[0][2], az[1][2]), fold2(az[0][3], az[1][3])));
        const unsigned a2 = safe(pk(fold2(az[2][0], az[3][0]), fold2(az[2][1], az[3][1]))), b2 = safe(pk(fold2(az[2][2], az[3][2]), fold2(az[2][3], az[3][3])));
        const auto ra = __builtin_amdgcn_permlane16_swap(a1, a2, false, false);
        const auto rb = __builtin_amdgcn_permlane16_swap(b1, b2, false, false);
        const v4u o4 = {ra[0], rb[0], ra[1], rb[1]};
        const v2u o1 = {safe(pk(swap32_sum(az[4][0]), swap32_sum(az[4][1]))), safe(pk(swap32_sum(az[4][2]), swap32_sum(az[4][3])))};
        ts.mark(4, on);
        {
            const unsigned zb = (s & 1) ? zb1 : zb0;
            st128so(ws, !last ? r4 : OOB, zb, o4);
            st64so(ws, !last ? r1 : OOB, zb, o1);
            st128so(ws, q4, OFF_ZMP + (unsigned)s * (40u * 8192u), o4);
            st64so(ws, q1, OFF_ZMP + (unsigned)s * (40u * 8192u), o1);
        }
        if (s >= 1 && tid == 0) cnt_add(P.ws, C_CNTZM + (s - 1) * 128);
        ts.mark(6, on);
    }
    drain(); bar();
    if (tid == 0) cnt_add(P.ws, C_CNTZM + (NSEG - 1) * 128);
    ts.dump(P, 32 + h, nfail);
}

__device__ __forceinline__ void role_red(const Params& P, lds_t L, int c, int cls, int np) {
    const int tid = threadIdx.x, lane = tid & 63, w = __builtin_amdgcn_readfirstlane(tid >> 6);
    const rsrc_t ws = mkrs(P.ws, WS_USED);
    lds_t ZB = L;
    lds_t BZ = L + 65536;
    volatile LAS int* zflag = (volatile LAS int*)(L + 65536 + 8192);
    const unsigned lo = lane * 16;
    const v4u ones = {0xFFFFFFFFu, 0xFFFFFFFFu, 0xFFFFFFFFu, 0xFFFFFFFFu};
    if (tid == 0) *zflag = 0;
    if (w == 1) {
        unsigned nfz = 0;
        for (int s = 0; s < NSEG - 1; ++s) {
            bar();
            const unsigned zbase = zm_ring(s & 1, c, np);
            if (!canary_wait(ws, can_off(2, cls, np), s + 1, P.dz, 0x380 + s)) return;
            v4u zm[8];
#pragma unroll
            for (int k = 0; k < 8; ++k) { zm[k] = ld128so(ws, lo + (k & 3) * 1024, zbase + (k >> 2) * 4096); __builtin_amdgcn_sched_barrier(0); }
            unsigned mz = 0;
#pragma unroll
            for (int k = 0; k < 8; ++k) mz = umax4(mz, zm[k]);
            if (!__all(mz != 0xFFFFFFFFu)) {
                ++nfz;
                unsigned spins = 0;
                for (;;) {
                    asm volatile("" ::: "memory");
                    for (int z = 0; z < P.retry; ++z) __builtin_amdgcn_s_sleep(1);
                    mz = 0;
#pragma unroll
                    for (int k = 0; k < 8; ++k) { if (!valid4(zm[k])) zm[k] = ld128so(ws, lo + (k & 3) * 1024, zbase + (k >> 2) * 4096); }
#pragma unroll
                    for (int k = 0; k < 8; ++k) mz = umax4(mz, zm[k]);
                    if (__all(mz != 0xFFFFFFFFu)) break;
                    if (!spin_more(ws, spins, 0x400 + s)) return;
                }
            }
#pragma unroll
            for (int k = 0; k < 8; ++k) {
                const auto xa = __builtin_amdgcn_permlane16_swap(zm[k].x, zm[k].z, false, false), xb2 = __builtin_amdgcn_permlane16_swap(zm[k].y, zm[k].w, false, false);
                *(LAS v4u*)(BZ + k * 1024 + lo) = v4u{xa[0], xa[1], xb2[0], xb2[1]};
            }
            asm volatile("s_waitcnt lgkmcnt(0)" ::: "memory");
            *zflag = s + 1;
#pragma unroll
            for (int k = 0; k < 8; ++k) st128so(ws, lo + (k & 3) * 1024, zbase + (k >> 2) * 4096, ones);
            drain();
        }
        if ((STAMPS || STAMPLITE) && lane == 0) ((unsigned*)(P.ws + OFF_DBG + 8 * MB))[((64 + c) * 8 + 1) * 16 + 14] = nfz;
        return;
    }
    if (w != 0) {
        const v4u wsd = mksrd(P.ws, WS_USED);
        for (int s = 0; s < NSEG - 1; ++s) {
            if (w >= 4) {
                if (!wait_cnt(ws, C_CNTZ + s * 128, 32, 0x300 + s)) return;
#pragma unroll
                for (int k = 0; k < 8; ++k) dma16s(wsd, ZB + (s & 1) * 32768 + (8 * (w - 4) + k) * 1024, OFF_ZX + (unsigned)(s * 40 + c) * 32768u + (8 * (w - 4) + k) * 1024 + lane * 16);
                drain();
            }
            bar();
        }
        return;
    }
    const int sidx = ((lane >> 3) & 1) ? 0xEEEE : 0x4444;
    const unsigned mo = (unsigned)(lane >> 4) * (2 * 16384) + fragoff(c >> 2) + (c & 3) * 256 + ((lane & 15) ^ (4 * (c & 1))) * 16;
    const unsigned cano = (c == 0 && lane < 4) ? can_off(0, lane, np) : OOB;
    unsigned nfail = 0;
    TS ts; ts.init();
    for (int s = 0; s < NSEG - 1; ++s) {
        const bool on = s >= 8 && s < 60;
        bar();
        ts.mark(0, on);
        const int sl = s & 1;
        lds_t zb = ZB + sl * 32768 + lane * 16;
        const unsigned pbase = p_base(sl, cls - 1, np);
        f32x4 acc0, acc1;
        unsigned spins = 0; bool first = true;
        for (;;) {
            v4u B[32];
#pragma unroll
            for (int k = 0; k < 32; ++k) B[k] = *(LAS v4u*)(zb + k * 1024);
            if (first) { if (!canary_wait(ws, can_off(1, cls, np), s + 1, P.dr, 0x480 + s)) return; }
            asm volatile("" : "+v"(B[0]), "+v"(B[31]));
            ts.mark(1, on && first);
            if (STAMPS > 2 && first && s >= 30 && s < 38) { const unsigned t_ = rtime(); if (lane == 0) ((unsigned*)(P.ws + OFF_DBG + 9 * MB))[((64 + c) * 8 + (s - 30)) * 4 + 0] = t_; }
            v4u p[20];
#pragma unroll
            for (int f = 0; f < 16; ++f) { p[f] = ld128so(ws, lo + (f & 3) * 1024, pbase + (f >> 3) * 16384 + ((f >> 2) & 1) * 4096); __builtin_amdgcn_sched_barrier(0); }
#pragma unroll
            for (int f = 0; f < 4; ++f) { p[16 + f] = ld128so(ws, lo + f * 1024, pbase + 2 * 16384); __builtin_amdgcn_sched_barrier(0); }
            unsigned mp = 0;
            acc0 = f32x4{0.f, 0.f, 0.f, 0.f}; acc1 = acc0;
#pragma unroll
            for (int f = 0; f < 10; ++f) {
                const v4u x = B[2 * f], y = B[2 * f + 1];
                if (f & 1) acc1 = smf(p[f], v8u{x.x, x.y, x.z, x.w, y.x, y.y, y.z, y.w}, acc1, sidx); else acc0 = smf(p[f], v8u{x.x, x.y, x.z, x.w, y.x, y.y, y.z, y.w}, acc0, sidx);
                mp = umax4(mp, p[f]);
                if (f == RCAN) st32s(ws, cano, (unsigned)(s + 1));
                __builtin_amdgcn_sched_barrier(0);
            }
            { unsigned sp2 = 0; while (*zflag < s + 1) { if (++sp2 > (SPIN_CAP << 6)) { set_abort(ws, 0x600 + s); return; } if ((sp2 & 1023u) == 0u && aborted(ws)) return; } }
            v4u bz[8];
#pragma unroll
            for (int k = 0; k < 8; ++k) bz[k] = *(LAS v4u*)(BZ + k * 1024 + lo);
            __builtin_amdgcn_sched_barrier(0);
#pragma unroll
            for (int f = 10; f < 16; ++f) {
                const v4u x = B[2 * f], y = B[2 * f + 1];
                if (f & 1) acc1 = smf(p[f], v8u{x.x, x.y, x.z, x.w, y.x, y.y, y.z, y.w}, acc1, sidx); else acc0 = smf(p[f], v8u{x.x, x.y, x.z, x.w, y.x, y.y, y.z, y.w}, acc0, sidx);
                mp = umax4(mp, p[f]);
                if (f == RCAN) st32s(ws, cano, (unsigned)(s + 1));
                __builtin_amdgcn_sched_barrier(0);
            }
#pragma unroll
            for (int F = 0; F < 4; ++F) {
                const v4u x = bz[2 * F], y = bz[2 * F + 1];
                if (F & 1) acc1 = smf(p[16 + F], v8u{x.x, x.y, x.z, x.w, y.x, y.y, y.z, y.w}, acc1, sidx); else acc0 = smf(p[16 + F], v8u{x.x, x.y, x.z, x.w, y.x, y.y, y.z, y.w}, acc0, sidx);
                mp = umax4(mp, p[16 + F]);
                __builtin_amdgcn_sched_barrier(0);
            }
            if (__all(mp != 0xFFFFFFFFu)) break;
            if (first) ++nfail;
            first = false;
            for (int z = 0; z < P.retry; ++z) __builtin_amdgcn_s_sleep(1);
            if (!spin_more(ws, spins, 0x500 + s)) return;
        }
        ts.mark(2, on);
        acc0 += acc1;
        const unsigned d0 = safe(pk(swap32_sum(acc0[0]), swap32_sum(acc0[1]))), d1 = safe(pk(swap32_sum(acc0[2]), swap32_sum(acc0[3])));
        const auto r0 = __builtin_amdgcn_permlane16_swap(d0, d0, false, false);
        const auto r1 = __builtin_amdgcn_permlane16_swap(d1, d1, false, false);
        const v4u o = {r0[0], r1[0], r0[1], r1[1]};
        st128so(ws, mo, m_base((s + 1) % 3, 0, np), o);
        if (STAMPS > 2 && s >= 30 && s < 38) { const unsigned t_ = rtime(); if (lane == 0) ((unsigned*)(P.ws + OFF_DBG + 9 * MB))[((64 + c) * 8 + (s - 30)) * 4 + 1] = t_; }
        ts.mark(3, on);
        if (DEVDBG && (P.dbg & 1)) st128s(ws, lane < 16 ? OFF_DBG + (unsigned)(s + 1) * 10240u + (unsigned)c * 256 + lane * 16 : OOB, o);
        st128so(ws, s >= 1 ? mo : OOB, m_base(s % 3, 0, np), ones);
        ts.mark(4, on);
    }
    ts.dump(P, 64 + c, nfail);
}

__device__ __forceinline__ void stage_x(v4u rx, lds_t dst, int s, const unsigned (&xo)[5], int w) {
#pragma unroll
    for (int m = 0; m < 5; ++m) dma16(rx, dst + (w + 8 * m) * 1024, xo[m] + (unsigned)s * (32u * 1280u));
}
__device__ __forceinline__ void init_xo(unsigned (&xo)[5], int w, int lane) {
#pragma unroll
    for (int m = 0; m < 5; ++m) {
        const int S = (w + 8 * m) * 64 + lane, tok = S / 80, sp = S % 80;
        const int src = (sp & ~15) | ((sp & 15) ^ (tok & 15));
        xo[m] = (unsigned)(tok * 1280 + src * 16);
    }
}
__device__ __forceinline__ void role_xqk(const Params& P, lds_t L, int h) {
    const int tid = threadIdx.x, lane = tid & 63, w = __builtin_amdgcn_readfirstlane(tid >> 6);
    const rsrc_t ws = mkrs(P.ws, WS_USED);
    const v4u rq = mksrd(P.Wq, DM * 4096 * 2), rk = mksrd(P.Wk, DM * 4096 * 2), rx = mksrd(P.x, SEQ * DM * 2);
    v4u W[2][20];
    {
        auto stage = [&](int st) {
            lds_t b = L + (st & 1) * 65536;
            stage_rows256<1>(rq, b, 128 * st, h, w, lane); stage_rows256<1>(rq, b + 16384, 128 * st + 64, h, w, lane);
            stage_rows256<1>(rk, b + 32768, 128 * st, h, w, lane); stage_rows256<1>(rk, b + 49152, 128 * st + 64, h, w, lane);
        };
        stage(0); stage(1);
#pragma unroll
        for (int st = 0; st < 5; ++st) {
            if (st < 4) asm volatile("s_waitcnt vmcnt(8)" ::: "memory"); else drain();
            bar();
            lds_t b = L + (st & 1) * 65536 + (w >> 2) * 32768;
#pragma unroll
            for (int k = 0; k < 4; ++k)
#pragma unroll
                for (int mt = 0; mt < 2; ++mt) W[mt][4 * st + k] = rd_denseNat(b + (k >> 1) * 16384, 256, 32 * (k & 1), 2 * (w & 3) + mt, lane);
            bar();
            if (st + 2 < 5) stage(st + 2);
        }
    }
    unsigned xo[5]; init_xo(xo, w, lane);
    const int XT = 40960;
    stage_x(rx, L, 0, xo, w);
    unsigned bo[4];
#pragma unroll
    for (int k = 0; k < 4; ++k) bo[k] = (lane & 15) * 1280 + (((4 * k + (lane >> 4)) ^ (lane & 15)) * 16);
    const unsigned obase = (w < 4 ? OFF_QX : OFF_KX) + (w & 3) * 2048 + lane * 16;
    for (int s = 0; s < NSEG; ++s) {
        drain();
        bar();
        if (s + 1 < NSEG) stage_x(rx, L + ((s + 1) & 1) * XT, s + 1, xo, w);
        lds_t xb = L + (s & 1) * XT;
        f32x4 acc[2][2];
#pragma unroll
        for (int a = 0; a < 2; ++a)
#pragma unroll
            for (int b = 0; b < 2; ++b) acc[a][b] = f32x4{0.f, 0.f, 0.f, 0.f};
#pragma unroll
        for (int k = 0; k < 20; ++k)
#pragma unroll
            for (int t = 0; t < 2; ++t) {
                const v4u b = *(LAS v4u*)(xb + t * 16 * 1280 + (k >> 2) * 256 + bo[k & 3]);
                acc[0][t] = mf32(W[0][k], b, acc[0][t]);
                acc[1][t] = mf32(W[1][k], b, acc[1][t]);
            }
#pragma unroll
        for (int t = 0; t < 2; ++t)
            st128s(ws, obase + (unsigned)(s * 32 + h) * 8192u + t * 1024, v4u{pk(acc[0][t][0], acc[0][t][1]), pk(acc[0][t][2], acc[0][t][3]), pk(acc[1][t][0], acc[1][t][1]), pk(acc[1][t][2], acc[1][t][3])});
        asm volatile("s_waitcnt vmcnt(0)\n\ts_barrier" ::: "memory");
        if (tid == 0) cnt_add(P.ws, C_CNTX + s * 128);
    }
}
__device__ __forceinline__ void role_xv(const Params& P, lds_t L, int h) {
    const int tid = threadIdx.x, lane = tid & 63, w = __builtin_amdgcn_readfirstlane(tid >> 6);
    const rsrc_t ws = mkrs(P.ws, WS_USED);
    const v4u rv = mksrd(P.Wv, DM * 4096 * 2), ro = mksrd(P.Wo, 4096 * DM * 2), rx = mksrd(P.x, SEQ * DM * 2);
    v4u Wv[20], Wo[5][4];
    {
        auto stage = [&](int st) { lds_t b = L + (st & 1) * 32768; stage_rows256<1>(rv, b, 128 * st, h, w, lane); stage_rows256<1>(rv, b + 16384, 128 * st + 64, h, w, lane); };
        stage(0); stage(1);
        stage_rows1280<2>(ro, L + 65536, 0, h, w, lane);
#pragma unroll
        for (int st = 0; st < 5; ++st) {
            drain();
            bar();
            lds_t b = L + (st & 1) * 32768;
#pragma unroll
            for (int k = 0; k < 4; ++k) Wv[4 * st + k] = rd_denseNat(b + (k >> 1) * 16384, 256, 32 * (k & 1), w, lane);
            if (st < 2) {
#pragma unroll
                for (int t = 0; t < 5; ++t)
#pragma unroll
                    for (int k = 0; k < 2; ++k) Wo[t][2 * st + k] = rd_densePerm(L + 65536, 1280, 32 * k, 5 * w + t, lane);
            }
            bar();
            if (st + 2 < 5) stage(st + 2);
            if (st == 0) stage_rows1280<2>(ro, L + 65536, 64, h, w, lane);
        }
    }
    unsigned xo[5]; init_xo(xo, w, lane);
    const int XT = 40960;
    lds_t VB = L + 2 * XT;
    stage_x(rx, L, 0, xo, w);
    unsigned bo[4];
#pragma unroll
    for (int k = 0; k < 4; ++k) bo[k] = (lane & 15) * 1280 + (((4 * k + (lane >> 4)) ^ (lane & 15)) * 16);
    for (int s = 0; s < NSEG; ++s) {
        drain();
        bar();
        if (s + 1 < NSEG) stage_x(rx, L + ((s + 1) & 1) * XT, s + 1, xo, w);
        lds_t xb = L + (s & 1) * XT;
        f32x4 av[2] = {f32x4{0.f, 0.f, 0.f, 0.f}, f32x4{0.f, 0.f, 0.f, 0.f}};
#pragma unroll
        for (int k = 0; k < 20; ++k)
#pragma unroll
            for (int t = 0; t < 2; ++t) av[t] = mf32(Wv[k], *(LAS v4u*)(xb + t * 16 * 1280 + (k >> 2) * 256 + bo[k & 3]), av[t]);
#pragma unroll
        for (int t = 0; t < 2; ++t) *(LAS v2u*)(VB + (t * 4 + (w >> 1)) * 1024 + lane * 16 + 8 * (w & 1)) = v2u{pk(av[t][0], av[t][1]), pk(av[t][2], av[t][3])};
        bar();
        f32x4 az[2][5];
#pragma unroll
        for (int t = 0; t < 2; ++t)
#pragma unroll
            for (int c = 0; c < 5; ++c) az[t][c] = f32x4{0.f, 0.f, 0.f, 0.f};
#pragma unroll
        for (int k = 0; k < 4; ++k)
#pragma unroll
            for (int t = 0; t < 2; ++t) {
                const v4u a = *(LAS v4u*)(VB + (t * 4 + k) * 1024 + lane * 16);
#pragma unroll
                for (int c = 0; c < 5; ++c) az[t][c] = mf32(a, Wo[c][k], az[t][c]);
            }
#pragma unroll
        for (int c = 0; c < 5; ++c)
            st128s(ws, OFF_ZX + (unsigned)(s * 40 + 5 * w + c) * 32768u + h * 1024 + lane * 16,
                   v4u{pk(az[0][c][0], az[0][c][1]), pk(az[0][c][2], az[0][c][3]), pk(az[1][c][0], az[1][c][1]), pk(az[1][c][2], az[1][c][3])});
        asm volatile("s_waitcnt vmcnt(0)\n\ts_barrier" ::: "memory");
        if (tid == 0) cnt_add(P.ws, C_CNTZ + s * 128);
    }
}

__device__ __forceinline__ float bf2f(unsigned short b) { return __builtin_bit_cast(float, (unsigned)b << 16); }
__device__ __forceinline__ float bfround(float f) { return asf(pk(f, 0.f) << 16); }
__device__ __forceinline__ f32x4 mff32(float a, float b, f32x4 c) { return __builtin_amdgcn_mfma_f32_16x16x4f32(a, b, c, 0, 0, 0); }

__device__ __forceinline__ bool final_job(const Params& P, rsrc_t ws, lds_t L, int s, int cg) {
    const int tid = threadIdx.x, lane = tid & 63, w = __builtin_amdgcn_readfirstlane(tid >> 6);
    if (!wait_cnt(ws, C_CNTP + s * 128, 32, 0x700 + s)) return false;
    if (!wait_cnt(ws, C_CNTZM + s * 128, 32, 0x780 + s)) return false;
    if (!wait_cnt(ws, C_CNTZ + s * 128, 32, 0x7C0 + s)) return false;
    const unsigned lo = lane * 16;
    v4u ax[4][2], am[2], bx[4][4], bm[4];
#pragma unroll
    for (int hh = 0; hh < 4; ++hh) {
#pragma unroll
        for (int t = 0; t < 2; ++t) ax[hh][t] = ld128so(ws, lo + t * 1024, OFF_PXX + (unsigned)(s * 32 + 4 * w + hh) * 2048u);
#pragma unroll
        for (int c = 0; c < 4; ++c) bx[hh][c] = ld128so(ws, lo, OFF_ZX + (unsigned)(s * 40 + 4 * cg + c) * 32768u + (4 * w + hh) * 1024);
    }
#pragma unroll
    for (int t = 0; t < 2; ++t) am[t] = ld128so(ws, (lane >> 4) * 512 + (16 * t + (lane & 15)) * 16, OFF_PXM + (unsigned)(s * 32 + 4 * w) * 512u);
#pragma unroll
    for (int c = 0; c < 4; ++c) bm[c] = ld128so(ws, lo, OFF_ZMP + (unsigned)(s * 40 + 4 * cg + c) * 8192u + 4 * w * 256);
    f32x4 acc[2][4];
#pragma unroll
    for (int t = 0; t < 2; ++t)
#pragma unroll
        for (int c = 0; c < 4; ++c) acc[t][c] = f32x4{0.f, 0.f, 0.f, 0.f};
#pragma unroll
    for (int hh = 0; hh < 4; ++hh)
#pragma unroll
        for (int t = 0; t < 2; ++t)
#pragma unroll
            for (int c = 0; c < 4; ++c) acc[t][c] = mf32(ax[hh][t], bx[hh][c], acc[t][c]);
#pragma unroll
    for (int t = 0; t < 2; ++t)
#pragma unroll
        for (int c = 0; c < 4; ++c) acc[t][c] = mf32(am[t], bm[c], acc[t][c]);
    bar();
#pragma unroll
    for (int t = 0; t < 2; ++t)
#pragma unroll
        for (int c = 0; c < 4; ++c) *(LAS f32x4*)(L + ((w * 2 + t) * 4 + c) * 1024 + lo) = acc[t][c];
    bar();
    f32x4 v = {0.f, 0.f, 0.f, 0.f};
#pragma unroll
    for (int ww = 0; ww < 8; ++ww) v += *(LAS f32x4*)(L + (ww * 8 + w) * 1024 + lo);
    lds_t OT = L + 65536;
    {
        const int tok = 16 * (w >> 2) + 4 * (lane >> 4), col = 16 * (w & 3) + (lane & 15);
#pragma unroll
        for (int r = 0; r < 4; ++r) *(LAS unsigned short*)(OT + (tok + r) * 144 + col * 2) = (unsigned short)(pk(v[r], 0.f) & 0xffffu);
    }
    bar();
    if (tid < 256) {
        const int row = tid >> 3, ch = tid & 7;
        const v4u o = *(LAS v4u*)(OT + row * 144 + ch * 16);
        *(v4u*)((char*)P.out + (size_t)(32 * s + row) * 2560 + 1280 + cg * 128 + ch * 16) = o;
    }
    return true;
}

__device__ __forceinline__ void ssm_job(const Params& P, lds_t L, int d0) {
    const int tid = threadIdx.x, lane = tid & 63, w = __builtin_amdgcn_readfirstlane(tid >> 6);
    constexpr int UPAD = 256, UST = (UPAD + 2048) * 2 + 32;
    lds_t UT = L;
    lds_t KD = L + 37376;
    lds_t PR = L + 58368;
    lds_t RV = L + 74752;
    lds_t TC = L + 79104;
    constexpr int TST = 4144;
    bar();
    for (int i = tid; i < 8 * (UPAD / 2); i += 512) *(LAS unsigned*)(UT + (i / (UPAD / 2)) * UST + (i % (UPAD / 2)) * 4) = 0u;
#pragma unroll
    for (int it = 0; it < 4; ++it) {
        const int l = tid + 512 * it;
        const v4u v = *(const v4u*)((const char*)P.u + (size_t)l * 1280 + d0 * 2);
        const unsigned e[4] = {v.x, v.y, v.z, v.w};
#pragma unroll
        for (int c = 0; c < 8; ++c) *(LAS unsigned short*)(UT + c * UST + (UPAD + l) * 2) = (unsigned short)((c & 1) ? e[c >> 1] >> 16 : e[c >> 1] & 0xffffu);
    }
    const int d = d0 + w;
    {
        const float dt = bfround(expf(bf2f(((const unsigned short*)P.logdt)[d])));
#pragma unroll
        for (int it = 0; it < 2; ++it) {
            const int n = lane + 64 * it, o = d * 128 + n;
            const float ar = dt * bf2f(((const unsigned short*)P.Are)[o]), ai = dt * bf2f(((const unsigned short*)P.Aim)[o]);
            const float br = dt * bf2f(((const unsigned short*)P.Bre)[o]), bi = dt * bf2f(((const unsigned short*)P.Bim)[o]);
            const float cr = bf2f(((const unsigned short*)P.Cre)[o]), ci = bf2f(((const unsigned short*)P.Cim)[o]);
            *(LAS f32x4*)(PR + (w * 128 + n) * 16) = f32x4{ar * 1.4426950408889634f, ai * 0.15915494309189535f, cr * br - ci * bi, cr * bi + ci * br};
        }
    }
    bar();
#pragma unroll 1
    for (int r = 0; r < 4; ++r) {
        const int step = 1 << r, base = r == 0 ? 0 : 128 << r;
        const float la = (float)(base + 16 * step * (lane & 15)), lb = (float)(step * (lane & 15));
        f32x4 a0 = {0.f, 0.f, 0.f, 0.f}, a1 = a0;
#pragma unroll 2
        for (int i = 0; i < 32; ++i) {
            const f32x4 q = *(LAS f32x4*)(PR + (w * 128 + 4 * i + (lane >> 4)) * 16);
            const float ea = __builtin_amdgcn_exp2f(q[0] * la), eb = __builtin_amdgcn_exp2f(q[0] * lb);
            const float pa = __builtin_amdgcn_fractf(q[1] * la), pb = __builtin_amdgcn_fractf(q[1] * lb);
            const float ca = ea * __builtin_amdgcn_cosf(pa), sa = ea * __builtin_amdgcn_sinf(pa);
            const float er = eb * __builtin_amdgcn_cosf(pb), ei = eb * __builtin_amdgcn_sinf(pb);
            const float fr = q[2] * ca - q[3] * sa, fi = q[2] * sa + q[3] * ca;
            a0 = mff32(fr, er, a0); a1 = mff32(-fi, ei, a1);
        }
        a0 += a1;
        const int kb = r == 0 ? 0 : 128 + 128 * r;
#pragma unroll
        for (int rr = 0; rr < 4; ++rr) {
            const int j = 16 * (4 * (lane >> 4) + rr) + (lane & 15);
            if (r == 0 || j < 128 || (r == 3 && j == 128)) *(LAS float*)(KD + (w * 648 + kb + j) * 4) = a0[rr];
        }
    }
    bar();
    const int tile = w < 4 ? w : 11 - w;
    const int b = lane & 15, g = lane >> 4, sg = (8 - (b & 7)) & 7;
    lds_t ap = TC + sg * TST + (2048 - 16 * (g >> 1) - (b + sg) + 8 * (g & 1)) * 2;
    const float dsk_unused = 0.f; (void)dsk_unused;
#pragma unroll 1
    for (int c = 0; c < 8; ++c) {
        for (int x = tid; x < 2080; x += 512) {
            const int m = 2048 - x;
            float kv = 0.f;
            if (m >= 0 && m < 2048) {
                const int lt = (int)bfround((float)m);
                const int ix = lt < 256 ? lt : lt < 512 ? 256 + ((lt - 256) >> 1) : lt < 1024 ? 384 + ((lt - 512) >> 2) : 512 + ((lt - 1024) >> 3);
                kv = *(LAS float*)(KD + (c * 648 + ix) * 4);
            }
            *(LAS unsigned short*)(RV + x * 2) = (unsigned short)(pk(kv, 0.f) & 0xffffu);
        }
        bar();
        if (tid < 258) {
            const v4u p0 = *(LAS v4u*)(RV + tid * 16), p1 = *(LAS v4u*)(RV + tid * 16 + 16);
            const unsigned e[8] = {p0.x, p0.y, p0.z, p0.w, p1.x, p1.y, p1.z, p1.w};
#pragma unroll
            for (int sgm = 0; sgm < 8; ++sgm) {
                v4u o;
#pragma unroll
                for (int k = 0; k < 4; ++k) o[k] = (sgm & 1) ? __builtin_amdgcn_alignbit(e[(sgm >> 1) + k + 1], e[(sgm >> 1) + k], 16) : e[(sgm >> 1) + k];
                *(LAS v4u*)(TC + sgm * TST + tid * 16) = o;
            }
        }
        bar();
        lds_t bp = UT + c * UST + (UPAD + 256 * tile + 16 * b - 16 * (g >> 1) + 8 * (g & 1)) * 2;
        f32x4 y0 = {0.f, 0.f, 0.f, 0.f}, y1 = y0;
        const int ne = 8 * tile + 8;
#pragma unroll 4
        for (int e = 0; e < ne; e += 2) {
            y0 = mf32(*(LAS v4u*)(ap - 64 * e), *(LAS v4u*)(bp - 64 * e), y0);
            y1 = mf32(*(LAS v4u*)(ap - 64 * e - 64), *(LAS v4u*)(bp - 64 * e - 64), y1);
        }
        y0 += y1;
        const int l0 = 16 * (16 * tile + b) + 4 * g;
        lds_t up = UT + c * UST + (UPAD + l0) * 2;
        const v2u uu = *(LAS v2u*)up;
        const float dsk = bf2f(((const unsigned short*)P.Dsk)[d0 + c]);
        const float u0 = asf(uu.x << 16), u1 = asf(uu.x & 0xffff0000u), u2 = asf(uu.y << 16), u3 = asf(uu.y & 0xffff0000u);
        const v2u o = {pk(bfround(y0[0]) + bfround(u0 * dsk), bfround(y0[1]) + bfround(u1 * dsk)), pk(bfround(y0[2]) + bfround(u2 * dsk), bfround(y0[3]) + bfround(u3 * dsk))};
        bar();
        *(LAS v2u*)up = o;
    }
    bar();
#pragma unroll
    for (int it = 0; it < 4; ++it) {
        const int l = tid + 512 * it;
        unsigned e[8];
#pragma unroll
        for (int c = 0; c < 8; ++c) e[c] = *(LAS unsigned short*)(UT + c * UST + (UPAD + l) * 2);
        *(v4u*)((char*)P.out + (size_t)l * 2560 + d0 * 2) = v4u{e[0] | (e[1] << 16), e[2] | (e[3] << 16), e[4] | (e[5] << 16), e[6] | (e[7] << 16)};
    }
}
constexpr int NG = 64;
__device__ __forceinline__ void role_g(const Params& P, lds_t L, int g) {
    if (P.rolemask & 32) { for (int j = g; j < 80; j += NG) ssm_job(P, L, 8 * j); }
    if (P.rolemask & 64) {
        const rsrc_t ws = mkrs(P.ws, WS_USED);
        for (int j = g; j < NSEG * 10; j += NG) { if (!final_job(P, ws, L, j / 10, j % 10)) return; }
    }
}

__device__ __forceinline__ int claim_role(const Params& P, lds_t L) {
    if (threadIdx.x == 0) {
        const rsrc_t ws = mkrs(P.ws, WS_USED);
        int role = blockIdx.x;
        if (!(P.dbg & 8)) {
            const unsigned xcc = (unsigned)__builtin_amdgcn_s_getreg(20 | (3 << 11)) & 7u;
            const unsigned t = __hip_atomic_fetch_add((GAS unsigned*)(P.ws + C_TICK + xcc * 128), 1u, __ATOMIC_RELAXED, __HIP_MEMORY_SCOPE_AGENT) + 1u;
            asm volatile("s_waitcnt vmcnt(0)" ::: "memory");
            __hip_atomic_fetch_add((GAS unsigned*)(P.ws + C_ARR), 1u, __ATOMIC_RELAXED, __HIP_MEMORY_SCOPE_AGENT);
            if (t < 32u) role = (int)(xcc * 32 + t);
            else {
                role = -1;
                unsigned spins = 0;
                for (;;) { const unsigned a = ld32s(ws, C_ARR) + 1u; if (a >= 256u) break; __builtin_amdgcn_s_sleep(4); if (++spins > SPIN_CAP) { set_abort(ws, 0x10); break; } }
                if (spins <= SPIN_CAP) {
                    unsigned k = __hip_atomic_fetch_add((GAS unsigned*)(P.ws + C_OVF), 1u, __ATOMIC_RELAXED, __HIP_MEMORY_SCOPE_AGENT) + 1u;
                    for (int j = 0; j < 8; ++j) { const unsigned n = ld32s(ws, C_TICK + j * 128) + 1u, fr = n < 32u ? 32u - n : 0u; if (role < 0) { if (k < fr) role = j * 32 + (int)(n + k); else k -= fr; } }
                }
            }
        } else role = (blockIdx.x & 7) * 32 + (blockIdx.x >> 3);
        *(LAS int*)L = role;
    }
    __syncthreads();
    const int role = __builtin_amdgcn_readfirstlane(*(LAS int*)L);
    __syncthreads();
    return role;
}
__device__ __forceinline__ int near_parity(const Params& P, bool leader) {
    const rsrc_t ws = mkrs(P.ws, WS_USED);
    if (P.np >= 0) return P.np;
    if (leader && threadIdx.x < 64) {
        unsigned t[2];
#pragma unroll
        for (int par = 0; par < 2; ++par) {
            unsigned best = 0xFFFFFFFFu;
#pragma unroll
            for (int r = 0; r < 4; ++r) {
                unsigned long long c0, c1; unsigned v;
                asm volatile("s_memtime %0\n\ts_waitcnt lgkmcnt(0)" : "=s"(c0) :: "memory");
                v = ld32s(ws, C_PROBE + par * BLK + r * 1024); asm volatile("s_waitcnt vmcnt(0)" : "+v"(v) :: "memory");
                asm volatile("s_memtime %0\n\ts_waitcnt lgkmcnt(0)" : "=s"(c1) :: "memory");
                const unsigned d = (unsigned)(c1 - c0); if (r > 0 && d < best) best = d;
            }
            t[par] = best;
        }
        const unsigned a0 = ((C_PROBE >> 13) & 1);
        const unsigned np = t[0] <= t[1] ? a0 : (a0 ^ 1);
        if (threadIdx.x == 0) st32s(ws, C_NP, np);
    }
    unsigned spins = 0;
    for (;;) { asm volatile("" ::: "memory"); const unsigned v = __builtin_amdgcn_readfirstlane(ld32s(ws, C_NP)); if (v != 0xFFFFFFFFu) return (int)(v & 1); __builtin_amdgcn_s_sleep(2); if (!spin_more(ws, spins, 0x20)) return 0; }
}

__global__ void __launch_bounds__(512, 2) k_main(Params P) {
    extern __shared__ __attribute__((aligned(16))) char smem[];
    lds_t L = (lds_t)smem;
    const int role = claim_role(P, L);
    if (role < 0) return;
    const int cls = role >> 5, r = role & 31;
    if (cls == 0) { if (P.rolemask & 1) role_qk(P, L, r, near_parity(P, r == 0)); }
    else if (cls <= 3) {
        const int nv = cls == 3 ? 10 : 11;
        if (r < nv) { if (P.rolemask & 2) role_vo(P, L, 11 * (cls - 1) + r, cls, near_parity(P, false)); }
        else if (r < 24) { if (P.rolemask & 4) role_red(P, L, 13 * (cls - 1) + (r - nv), cls, near_parity(P, false)); }
    }
    else if (cls == 4) { if (P.rolemask & 8) role_xqk(P, L, r); }
    else if (cls == 5) { if (P.rolemask & 16) role_xv(P, L, r); }
    else role_g(P, L, (cls - 6) * 32 + r);
}

}

extern "C" void kernel_launch(void* const* d_in, const int* in_sizes, int n_in, void* d_out, int out_size, void* d_ws, size_t ws_size, hipStream_t stream) {
    char* base = (char*)(((uintptr_t)d_ws + (2u << 20) - 1) & ~(uintptr_t)((2u << 20) - 1));
    Params P;
    P.x = d_in[9]; P.mem0 = d_in[10]; P.Wq = d_in[11]; P.Wk = d_in[12]; P.Wv = d_in[13]; P.Wo = d_in[14];
    P.u = d_in[0]; P.Are = d_in[1]; P.Aim = d_in[2]; P.Bre = d_in[3]; P.Bim = d_in[4]; P.Cre = d_in[5]; P.Cim = d_in[6]; P.logdt = d_in[7]; P.Dsk = d_in[8];
    P.ws = base; P.out = d_out; P.np = -1; P.dbg = 0; P.pad0 = 0; P.dq = TUNE_DQ; P.dr = TUNE_DR; P.dz = TUNE_DZ; P.retry = TUNE_RETRY; P.rolemask = 127;
    (void)hipFuncSetAttribute((const void*)k_main, hipFuncAttributeMaxDynamicSharedMemorySize, LDS_BYTES);
    (void)hipMemsetAsync(base, 0xFF, MEMSET_BYTES, stream);
    hipLaunchKernelGGL(k_main, dim3(256), dim3(512), LDS_BYTES, stream, P);
}
```

```cpp
#include <hip/hip_runtime.h>
#include <hip/hip_bf16.h>

namespace {
constexpr int TUNE_DQ = 12, TUNE_DR = 6, TUNE_DZ = 12, TUNE_RETRY = 2, TUNE_CQ = 0, TUNE_DZX = 0;

#define LAS __attribute__((address_space(3)))
#define GAS __attribute__((address_space(1)))
typedef unsigned v4u __attribute__((ext_vector_type(4)));
typedef unsigned v2u __attribute__((ext_vector_type(2)));
typedef float f32x4 __attribute__((ext_vector_type(4)));
typedef float f32x2 __attribute__((ext_vector_type(2)));
typedef short s16x4 __attribute__((ext_vector_type(4)));
typedef __bf16 bf16x2 __attribute__((ext_vector_type(2)));
typedef __bf16 bf16x8 __attribute__((ext_vector_type(8)));
typedef __bf16 bf16x16 __attribute__((ext_vector_type(16)));
typedef unsigned v8u __attribute__((ext_vector_type(8)));
typedef __amdgpu_buffer_rsrc_t rsrc_t;
typedef LAS char* lds_t;

constexpr int NSEG = 64, DM = 640, NH = 32, DH = 128, SEQ = 2048;
constexpr unsigned BLK = 8192;
constexpr unsigned J_ZM = 0, J_M = 80, J_PX = 104, J_PM = 120, J_END = 124;
constexpr unsigned OFF_CTL = 2 * J_END * BLK;
constexpr unsigned MEMSET_BYTES = OFF_CTL + 6 * BLK;
constexpr unsigned C_ABORT = OFF_CTL, C_NP = OFF_CTL + 128, C_TICK = OFF_CTL + 256  , C_ARR = OFF_CTL + 256 + 8 * 128, C_OVF = C_ARR + 128;
constexpr unsigned C_CNTX = OFF_CTL + 2048, C_CNTZ = C_CNTX + 64 * 128, C_CNTP = C_CNTZ + 64 * 128, C_CNTZM = C_CNTP + 64 * 128;
constexpr unsigned C_PROBE = OFF_CTL + 5 * BLK - 4096;
static_assert(C_CNTZM + 64 * 128 <= C_PROBE && C_PROBE + BLK + 4096 <= MEMSET_BYTES, "ctl");
constexpr unsigned MB = 1u << 20;
constexpr unsigned OFF_KX = 2 * MB, OFF_QX = 18 * MB, OFF_ZX = 34 * MB, OFF_ZMP = 114 * MB, OFF_PXX = 134 * MB, OFF_PXM = 138 * MB, OFF_DBG = 140 * MB, OFF_SSM = 150 * MB;
constexpr unsigned WS_USED = 200 * MB;
constexpr int AUX_SC1 = 16;
constexpr unsigned OOB = 0x80000000u;
constexpr unsigned SPIN_CAP = 1u << 15;
constexpr int LDS_BYTES = 152 * 1024;

struct Params {
    const void* x; const void* mem0; const void* Wq; const void* Wk; const void* Wv; const void* Wo;
    const void* u; const void* Are; const void* Aim; const void* Bre; const void* Bim; const void* Cre; const void* Cim; const void* logdt; const void* Dsk;
    char* ws;
    void* out;
    int rolemask; int np; int dbg; int dq; int dr; int dz; int retry; int cq; int dzx; int pad0;
};

__device__ __forceinline__ rsrc_t mkrs(const void* p, unsigned bytes) { return __builtin_amdgcn_make_buffer_rsrc((void*)p, 0, bytes, 0x00020000); }
__device__ __forceinline__ v4u ld128(rsrc_t rs, unsigned off, int aux) { return aux ? __builtin_amdgcn_raw_buffer_load_b128(rs, off, 0, AUX_SC1) : __builtin_amdgcn_raw_buffer_load_b128(rs, off, 0, 0); }
__device__ __forceinline__ v4u ld128s(rsrc_t rs, unsigned off) { return __builtin_amdgcn_raw_buffer_load_b128(rs, off, 0, AUX_SC1); }
__device__ __forceinline__ v2u ld64s(rsrc_t rs, unsigned off) { return __builtin_amdgcn_raw_buffer_load_b64(rs, off, 0, AUX_SC1); }
__device__ __forceinline__ unsigned ld32s(rsrc_t rs, unsigned off) { return __builtin_amdgcn_raw_buffer_load_b32(rs, off, 0, AUX_SC1); }
__device__ __forceinline__ v4u ld128so(rsrc_t rs, unsigned voff, unsigned soff) { return __builtin_amdgcn_raw_buffer_load_b128(rs, voff, soff, AUX_SC1); }
__device__ __forceinline__ v2u ld64so(rsrc_t rs, unsigned voff, unsigned soff) { return __builtin_amdgcn_raw_buffer_load_b64(rs, voff, soff, AUX_SC1); }
__device__ __forceinline__ void st128so(rsrc_t rs, unsigned voff, unsigned soff, v4u v) { __builtin_amdgcn_raw_buffer_store_b128(v, rs, voff, soff, AUX_SC1); }
__device__ __forceinline__ void st64so(rsrc_t rs, unsigned voff, unsigned soff, v2u v) { __builtin_amdgcn_raw_buffer_store_b64(v, rs, voff, soff, AUX_SC1); }
__device__ __forceinline__ unsigned umax3(unsigned a, unsigned b, unsigned c) { return max(max(a, b), c); }
__device__ __forceinline__ unsigned umax4(unsigned m, v4u v) { return umax3(umax3(m, v.x, v.y), v.z, v.w); }
__device__ __forceinline__ void st128s(rsrc_t rs, unsigned off, v4u v) { __builtin_amdgcn_raw_buffer_store_b128(v, rs, off, 0, AUX_SC1); }
__device__ __forceinline__ void st64s(rsrc_t rs, unsigned off, v2u v) { __builtin_amdgcn_raw_buffer_store_b64(v, rs, off, 0, AUX_SC1); }
__device__ __forceinline__ void st32s(rsrc_t rs, unsigned off, unsigned v) { __builtin_amdgcn_raw_buffer_store_b32(v, rs, off, 0, AUX_SC1); }
__device__ __forceinline__ v4u mksrd(const void* p, unsigned bytes) { const unsigned long long a = (unsigned long long)p; return v4u{(unsigned)a, (unsigned)(a >> 32) & 0xffffu, bytes, 0x00020000u}; }
__device__ __forceinline__ void dma16(v4u srd, lds_t dst, unsigned voff) {
    unsigned keep; const unsigned la = (unsigned)(unsigned long long)dst;
    asm volatile("s_mov_b32 %0, m0\n\ts_mov_b32 m0, %3\n\ts_nop 0\n\tbuffer_load_dwordx4 %1, %2, 0 offen lds\n\ts_mov_b32 m0, %0" : "=&s"(keep) : "v"(voff), "s"(srd), "s"(la) : "memory");
}
__device__ __forceinline__ void dma16s(v4u srd, lds_t dst, unsigned voff, unsigned soff) {
    unsigned keep; const unsigned la = (unsigned)(unsigned long long)dst;
    asm volatile("s_mov_b32 %0, m0\n\ts_mov_b32 m0, %3\n\ts_nop 0\n\tbuffer_load_dwordx4 %1, %2, %4 offen sc1 lds\n\ts_mov_b32 m0, %0" : "=&s"(keep) : "v"(voff), "s"(srd), "s"(la), "s"(soff) : "memory");
}
__device__ __forceinline__ void bar() { asm volatile("s_waitcnt lgkmcnt(0)\n\ts_barrier" ::: "memory"); }
__device__ __forceinline__ void drain() { asm volatile("s_waitcnt vmcnt(0)" ::: "memory"); }
__device__ __forceinline__ unsigned pk(float a, float b) { f32x2 v = {a, b}; return __builtin_bit_cast(unsigned, __builtin_convertvector(v, bf16x2)); }
__device__ __forceinline__ v4u mk_ones() { v4u o; asm volatile("v_mov_b32 %0, -1\n\tv_mov_b32 %1, -1\n\tv_mov_b32 %2, -1\n\tv_mov_b32 %3, -1" : "=v"(o.x), "=v"(o.y), "=v"(o.z), "=v"(o.w)); return o; }
__device__ __forceinline__ unsigned safe(unsigned x) { return x < 0xFFFFFFFEu ? x : 0xFFFFFFFEu; }
__device__ __forceinline__ bool valid4(v4u v) { return (v.x != 0xFFFFFFFFu) & (v.y != 0xFFFFFFFFu) & (v.z != 0xFFFFFFFFu) & (v.w != 0xFFFFFFFFu); }
__device__ __forceinline__ bool valid2(v2u v) { return (v.x != 0xFFFFFFFFu) & (v.y != 0xFFFFFFFFu); }
__device__ __forceinline__ v2u trd(lds_t p) { return __builtin_bit_cast(v2u, __builtin_amdgcn_ds_read_tr16_b64_v4i16((LAS s16x4*)p)); }
__device__ __forceinline__ f32x4 smf(v4u a, v8u b, f32x4 c, int idx) { return __builtin_amdgcn_smfmac_f32_16x16x64_bf16(__builtin_bit_cast(bf16x8, a), __builtin_bit_cast(bf16x16, b), c, idx, 0, 0); }
__device__ __forceinline__ f32x4 mf32(v4u a, v4u b, f32x4 c) { return __builtin_amdgcn_mfma_f32_16x16x32_bf16(__builtin_bit_cast(bf16x8, a), __builtin_bit_cast(bf16x8, b), c, 0, 0, 0); }
__device__ __forceinline__ f32x4 mf16(v2u a, v2u b, f32x4 c) { return __builtin_amdgcn_mfma_f32_16x16x16bf16_1k(__builtin_bit_cast(s16x4, a), __builtin_bit_cast(s16x4, b), c, 0, 0, 0); }
template <int CTRL> __device__ __forceinline__ float dppf(float v) { return __builtin_bit_cast(float, __builtin_amdgcn_update_dpp(0, __builtin_bit_cast(int, v), CTRL, 0xf, 0xf, true)); }
template <int CTRL> __device__ __forceinline__ unsigned dppu(unsigned v) { return (unsigned)__builtin_amdgcn_update_dpp(0, (int)v, CTRL, 0xf, 0xf, true); }
__device__ __forceinline__ float asf(unsigned u) { return __builtin_bit_cast(float, u); }
__device__ __forceinline__ unsigned asu(float f) { return __builtin_bit_cast(unsigned, f); }
__device__ __forceinline__ float swap32_sum(float x) { auto r = __builtin_amdgcn_permlane32_swap(asu(x), asu(x), false, false); const unsigned a = r[0], b = r[1]; return asf(a) + asf(b); }
__device__ __forceinline__ float swap32_max(float x) { auto r = __builtin_amdgcn_permlane32_swap(asu(x), asu(x), false, false); const unsigned a = r[0], b = r[1]; return fmaxf(asf(a), asf(b)); }
__device__ __forceinline__ float swap16_sum(float x) { auto r = __builtin_amdgcn_permlane16_swap(asu(x), asu(x), false, false); const unsigned a = r[0], b = r[1]; return asf(a) + asf(b); }
__device__ __forceinline__ float swap16_max(float x) { auto r = __builtin_amdgcn_permlane16_swap(asu(x), asu(x), false, false); const unsigned a = r[0], b = r[1]; return fmaxf(asf(a), asf(b)); }
__device__ __forceinline__ float fold2(float a, float b) { auto r = __builtin_amdgcn_permlane32_swap(asu(a), asu(b), false, false); const unsigned x = r[0], y = r[1]; return asf(x) + asf(y); }
__device__ __forceinline__ float vmax(float a, float b) { float r; asm("v_max_f32 %0, %1, %2" : "=v"(r) : "v"(a), "v"(b)); return r; }
__device__ __forceinline__ float vmax3(float a, float b, float c) { float r; asm("v_max3_f32 %0, %1, %2, %3" : "=v"(r) : "v"(a), "v"(b), "v"(c)); return r; }
__device__ __forceinline__ float row_max3(float x) {
    float y;
    asm("s_nop 1\n\tv_max_f32_dpp %0, %1, %1 row_ror:8 row_mask:0xf bank_mask:0xf\n\ts_nop 1" : "=&v"(y) : "v"(x));
    { auto r = __builtin_amdgcn_permlane16_swap(asu(y), asu(y), false, false); const unsigned a = r[0], b = r[1]; y = vmax(asf(a), asf(b)); }
    { auto r = __builtin_amdgcn_permlane32_swap(asu(y), asu(y), false, false); const unsigned a = r[0], b = r[1]; y = vmax(asf(a), asf(b)); }
    return y;
}
__device__ __forceinline__ float wave_max(float x) {
    float y;
    asm("s_nop 1\n\tv_max_f32_dpp %0, %1, %1 row_ror:8 row_mask:0xf bank_mask:0xf\n\ts_nop 1\n\tv_max_f32_dpp %0, %0, %0 row_ror:4 row_mask:0xf bank_mask:0xf\n\ts_nop 1\n\t"
        "v_max_f32_dpp %0, %0, %0 row_ror:2 row_mask:0xf bank_mask:0xf\n\ts_nop 1\n\tv_max_f32_dpp %0, %0, %0 row_ror:1 row_mask:0xf bank_mask:0xf\n\ts_nop 1" : "=&v"(y) : "v"(x));
    { auto r = __builtin_amdgcn_permlane16_swap(asu(y), asu(y), false, false); const unsigned a = r[0], b = r[1]; y = vmax(asf(a), asf(b)); }
    { auto r = __builtin_amdgcn_permlane32_swap(asu(y), asu(y), false, false); const unsigned a = r[0], b = r[1]; y = vmax(asf(a), asf(b)); }
    return y;
}
__device__ __forceinline__ float wave_sum(float x) { x += dppf<0x128>(x); x += dppf<0x124>(x); x += dppf<0x122>(x); x += dppf<0x121>(x); x = swap16_sum(x); return swap32_sum(x); }

struct Ctx {
    rsrc_t ws; lds_t L; int lane, w; int np;
};
__device__ __forceinline__ unsigned nblk(int j, int np) { return (unsigned)(2 * j + np) * BLK; }
__device__ __forceinline__ unsigned zm_ring(int slot, int c, int np) { return nblk(J_ZM + slot * 40 + c, np); }
__device__ __forceinline__ unsigned m_base(int slot, int copy, int np) { return nblk(J_M + 2 * (slot * 4 + copy), np); }
__device__ __forceinline__ unsigned px_base(int slot, int copy, int np) { return nblk(J_PX + 2 * (slot * 4 + copy), np); }
__device__ __forceinline__ unsigned pm_base(int slot, int copy, int np) { const int i = slot * 4 + copy; return nblk(J_PM + (i >> 1), np) + (i & 1) * 4096; }
__device__ __forceinline__ unsigned can_off(int kind, int cls, int np) { return nblk(J_M + 1, np) + 4096 + (kind * 4 + cls) * 128; }
__host__ __device__ constexpr unsigned fragoff(int f) { return (unsigned)((f >> 3) * 16384 + (f & 7) * 1024); }

__device__ __forceinline__ bool aborted(rsrc_t ws) { return __builtin_amdgcn_readfirstlane(ld32s(ws, C_ABORT)) != 0xFFFFFFFFu; }
__device__ __forceinline__ void set_abort(rsrc_t ws, unsigned code) { st32s(ws, C_ABORT, code); }
__device__ __forceinline__ bool spin_more(rsrc_t ws, unsigned& spins, unsigned code) {
    ++spins;
    if (spins > SPIN_CAP) { set_abort(ws, code); return false; }
    if ((spins & 63u) == 0u && aborted(ws)) return false;
    return true;
}
__device__ __forceinline__ bool wait_cnt(rsrc_t ws, unsigned off, unsigned n, unsigned code) {
    unsigned spins = 0;
    for (;;) {
        asm volatile("" ::: "memory");
        unsigned v = __builtin_amdgcn_readfirstlane(ld32s(ws, off));
        if (v + 1u >= n) return true;
        __builtin_amdgcn_s_sleep(2);
        if (!spin_more(ws, spins, code)) return false;
    }
}
#define DEVDBG 0
#define QCANK 10
#define RCAN 7
#define VCAN 1
#define RSTAMPS 0
#define SKEWST 0
#define STAMPLITE 0
#define STAMPS 0
constexpr int STAMP_OFF = 148 * 1024;
struct TS {
    unsigned acc[12]; unsigned last; unsigned r0, c0;
    __device__ __forceinline__ static unsigned now() { unsigned long long t; asm volatile("s_memtime %0\n\ts_waitcnt lgkmcnt(0)" : "=s"(t) :: "memory"); return (unsigned)t; }
    __device__ __forceinline__ void init() { if (STAMPLITE) r0 = (unsigned)__builtin_amdgcn_s_memrealtime(); if (STAMPS) { for (int i = 0; i < 12; ++i) acc[i] = 0; r0 = (unsigned)__builtin_amdgcn_s_memrealtime(); c0 = now(); last = c0; } }
    __device__ __forceinline__ void mark(int i, bool on = true) { if (STAMPS) { __builtin_amdgcn_sched_barrier(0); const unsigned t = now(); if (on) acc[i] += t - last; last = t; __builtin_amdgcn_sched_barrier(0); } }
    __device__ __forceinline__ void initR() { if (RSTAMPS) { for (int i = 0; i < 12; ++i) acc[i] = 0; last = now(); } }
    __device__ __forceinline__ void markR(int i, bool on) { if (RSTAMPS) { __builtin_amdgcn_sched_barrier(0); const unsigned t = now(); if (on) acc[i] += t - last; last = t; __builtin_amdgcn_sched_barrier(0); } }
    __device__ __forceinline__ void dumpR(const Params& P, int who) { if (RSTAMPS && (threadIdx.x & 63) == 0) { unsigned* o = (unsigned*)(P.ws + OFF_DBG + 11 * MB) + who * 16; for (int i = 0; i < 12; ++i) o[i] = acc[i]; } }
    __device__ __forceinline__ void dump(const Params& P, int who, unsigned extra) {
        if (STAMPLITE) { const unsigned r1 = (unsigned)__builtin_amdgcn_s_memrealtime(); const rsrc_t w_ = mkrs(P.ws, WS_USED); const unsigned vo = (threadIdx.x & 63) ? OOB : (threadIdx.x >> 6) * 64, so = OFF_DBG + 8 * MB + who * 512;
            __builtin_amdgcn_raw_buffer_store_b32(r1 - r0, w_, vo, so + 48, 0); __builtin_amdgcn_raw_buffer_store_b32(24 * (r1 - r0), w_, vo, so + 52, 0); __builtin_amdgcn_raw_buffer_store_b32(extra, w_, vo, so + 56, 0); }
        if (STAMPS) { const unsigned r1 = (unsigned)__builtin_amdgcn_s_memrealtime(), c1 = now();
            if ((threadIdx.x & 63) == 0) { unsigned* o = (unsigned*)(P.ws + OFF_DBG + 8 * MB) + (who * 8 + (threadIdx.x >> 6)) * 16; for (int i = 0; i < 12; ++i) o[i] = acc[i]; o[12] = r1 - r0; o[13] = c1 - c0; o[14] = extra; } }
    }
};
__device__ __forceinline__ void cnt_add(char* wsbase, unsigned off) { __hip_atomic_fetch_add((GAS unsigned*)(wsbase + off), 1u, __ATOMIC_RELAXED, __HIP_MEMORY_SCOPE_AGENT); }

__device__ __forceinline__ void gstamp(const Params& P, int role, int k) { if (STAMPLITE) { const unsigned t_ = (unsigned)__builtin_amdgcn_s_memrealtime(); __builtin_amdgcn_raw_buffer_store_b32(t_, mkrs(P.ws, WS_USED), threadIdx.x ? OOB : 0u, OFF_DBG + 10 * MB + (role * 8 + k) * 4, 0); } }
template <int SIG> __device__ __forceinline__ int sigma(int row) {
    if (SIG == 0) return (row & 1) | (((row >> 2) & 1) << 1) | (((row >> 3) & 1) << 2);
    if (SIG == 1) return (row & 3) | (((row >> 3) & 1) << 2);
    return row & 7;
}
template <int SIG> __device__ __forceinline__ void stage_rows256(v4u rw, lds_t dst, int row0, int h, int w, int lane) {
#pragma unroll
    for (int i = 0; i < 2; ++i) {
        const int ins = 2 * w + i, r = 4 * ins + (lane >> 4), s = lane & 15;
        const int col = (((s >> 1) ^ sigma<SIG>(r)) << 1) | (s & 1);
        dma16(rw, dst + ins * 1024, (unsigned)(row0 + r) * 8192u + (unsigned)h * 256u + (unsigned)col * 16u);
    }
}
template <int SIG> __device__ __forceinline__ void stage_rows1280(v4u rw, lds_t dst, int e0, int h, int w, int lane) {
#pragma unroll
    for (int i = 0; i < 10; ++i) {
        const int ins = 10 * w + i, S = ins * 64 + lane, r = S / 80, s = S % 80, s16 = s & 15;
        const int col = (s & ~15) | ((((s16 >> 1) ^ sigma<SIG>(r)) << 1) | (s16 & 1));
        dma16(rw, dst + ins * 1024, (unsigned)(h * 128 + e0 + r) * 1280u + (unsigned)col * 16u);
    }
}
__device__ __forceinline__ v8u rd_sparseB(lds_t base, int stride, int pair, int lane) {
    const int g = lane >> 4, q = (lane >> 2) & 3, p = lane & 3;
    v8u o;
#pragma unroll
    for (int b = 0; b < 2; ++b)
#pragma unroll
        for (int rho = 0; rho < 2; ++rho) {
            const int row = 32 * b + 16 * (g >> 1) + 4 * (g & 1) + 2 * rho + (q & 1) + 8 * (q >> 1);
            const v2u t = trd(base + row * stride + (pair >> 3) * 256 + (((pair & 7) ^ sigma<0>(row)) * 32) + 8 * p);
            o[4 * b + 2 * rho] = t.x; o[4 * b + 2 * rho + 1] = t.y;
        }
    return o;
}
__device__ __forceinline__ v4u rd_denseNat(lds_t base, int stride, int row0, int pair, int lane) {
    const int g = lane >> 4, q = (lane >> 2) & 3, p = lane & 3;
    v4u o;
#pragma unroll
    for (int rho = 0; rho < 2; ++rho) {
        const int row = row0 + 8 * g + 4 * rho + q;
        const v2u t = trd(base + row * stride + (pair >> 3) * 256 + (((pair & 7) ^ sigma<1>(row)) * 32) + 8 * p);
        o[2 * rho] = t.x; o[2 * rho + 1] = t.y;
    }
    return o;
}
__device__ __forceinline__ v4u rd_densePerm(lds_t base, int stride, int row0, int pair, int lane) {
    const int g = lane >> 4, q = (lane >> 2) & 3, p = lane & 3;
    v4u o;
#pragma unroll
    for (int rho = 0; rho < 2; ++rho) {
        const int row = row0 + 16 * rho + 4 * g + q;
        const v2u t = trd(base + row * stride + (pair >> 3) * 256 + (((pair & 7) ^ sigma<2>(row)) * 32) + 8 * p);
        o[2 * rho] = t.x; o[2 * rho + 1] = t.y;
    }
    return o;
}

__device__ __forceinline__ unsigned rtime() { return (unsigned)__builtin_amdgcn_s_memrealtime(); }
__device__ __forceinline__ void skst(rsrc_t ws, int lane, int idx) { const unsigned t_ = rtime(); __builtin_amdgcn_raw_buffer_store_b32(t_, ws, lane ? OOB : 0u, OFF_DBG + 9 * MB + idx * 4, 0); }
__device__ __forceinline__ bool canary_wait(rsrc_t ws, unsigned off, int want, int dly, unsigned code) {
    unsigned v0 = ld32s(ws, off); __builtin_amdgcn_s_sleep(1);
    unsigned v1 = ld32s(ws, off); __builtin_amdgcn_s_sleep(1);
    unsigned v2 = ld32s(ws, off); __builtin_amdgcn_s_sleep(1);
    unsigned v3 = ld32s(ws, off);
    unsigned spins = 0;
    for (;;) {
        if ((int)__builtin_amdgcn_readfirstlane(v0) >= want) break;
        asm volatile("" ::: "memory"); v0 = ld32s(ws, off);
        if ((int)__builtin_amdgcn_readfirstlane(v1) >= want) break;
        asm volatile("" ::: "memory"); v1 = ld32s(ws, off);
        if ((int)__builtin_amdgcn_readfirstlane(v2) >= want) break;
        asm volatile("" ::: "memory"); v2 = ld32s(ws, off);
        if ((int)__builtin_amdgcn_readfirstlane(v3) >= want) break;
        asm volatile("" ::: "memory"); v3 = ld32s(ws, off);
        if (!spin_more(ws, spins, code)) return false;
    }
    for (int z = 0; z < dly; ++z) asm volatile("s_nop 3");
    return true;
}

__device__ __forceinline__ void stage_mem0(const Params& P, lds_t Mb) {
    const rsrc_t rm = mkrs(P.mem0, 8 * DM * 2);
#pragma unroll
    for (int it = 0; it < 2; ++it) {
        const int d = threadIdx.x + 512 * it;
        if (d < DM) {
            unsigned v[8];
#pragma unroll
            for (int i = 0; i < 8; ++i) v[i] = __builtin_amdgcn_raw_buffer_load_b16(rm, (i * DM + d) * 2, 0, 0);
            *(LAS v4u*)(Mb + (d >> 4) * 256 + ((d & 15) ^ (4 * ((d >> 4) & 1))) * 16) = v4u{v[0] | (v[1] << 16), v[2] | (v[3] << 16), v[4] | (v[5] << 16), v[6] | (v[7] << 16)};
        }
    }
}
__device__ __forceinline__ bool gather_m(const Params& P, rsrc_t ws, lds_t Mb, int s, int cls, int w, int lane, int np, unsigned& nfail) {
    if (s == 0) return true;
    const bool two = w >= 6;
    const int f2 = w + 2;
    const unsigned sb = m_base(s % 3, cls, np);
    const unsigned oa = w * 1024 + lane * 16, ob = two ? 16384 + (w - 6) * 1024 + lane * 16 : OOB;
    if (w == 6) { if (!canary_wait(ws, can_off(0, cls, np), s, P.dq, 0x180 + s)) return false; }
    asm volatile("s_barrier" ::: "memory");
    v4u a = ld128so(ws, oa, sb), b = ld128so(ws, ob, sb);
    if (!__all(umax4(umax4(0u, a), b) != 0xFFFFFFFFu)) {
        unsigned spins = 0;
        ++nfail;
        for (;;) {
            asm volatile("" ::: "memory");
            for (int z = 0; z < P.retry; ++z) __builtin_amdgcn_s_sleep(1);
            if (!valid4(a)) a = ld128so(ws, oa, sb);
            if (!valid4(b)) b = ld128so(ws, ob, sb);
            if (__all(valid4(a) && valid4(b))) break;
            if (!spin_more(ws, spins, 0x100 + s)) return false;
        }
    }
    *(LAS v4u*)(Mb + w * 1024 + lane * 16) = a;
    if (two) *(LAS v4u*)(Mb + f2 * 1024 + lane * 16) = b;
    return true;
}
__device__ __forceinline__ unsigned afrag_off(int lane) { const int g = lane >> 4, q = (lane >> 2) & 3, p = lane & 3; return g * 256 + ((8 * (p >> 1) + q) ^ (4 * (g & 1))) * 16 + (p & 1) * 8; }
__device__ __forceinline__ v4u rd_afrag(lds_t base, unsigned a0, int k) { const v2u x = trd(base + k * 1024 + a0), y = trd(base + k * 1024 + (a0 ^ 64)); return v4u{x.x, x.y, y.x, y.y}; }

__device__ __forceinline__ void role_qk(const Params& P, lds_t L, int h, int cls, int np) {
    const int tid = threadIdx.x, lane = tid & 63, w = __builtin_amdgcn_readfirstlane(tid >> 6);
    const rsrc_t ws = mkrs(P.ws, WS_USED);
    const v4u rq = mksrd(P.Wq, DM * 4096 * 2), rk = mksrd(P.Wk, DM * 4096 * 2), wsd = mksrd(P.ws, WS_USED);
    v8u Wq[10], Wk[10];
    {
        auto stage = [&](int st) {
            lds_t b = L + (st & 1) * 65536;
            stage_rows256<0>(rq, b, 128 * st, h, w, lane); stage_rows256<0>(rq, b + 16384, 128 * st + 64, h, w, lane);
            stage_rows256<0>(rk, b + 32768, 128 * st, h, w, lane); stage_rows256<0>(rk, b + 49152, 128 * st + 64, h, w, lane);
        };
        stage(0); stage(1);
#pragma unroll
        for (int st = 0; st < 5; ++st) {
            if (st < 4) asm volatile("s_waitcnt vmcnt(8)" ::: "memory"); else drain();
            bar();
            lds_t b = L + (st & 1) * 65536;
            Wq[2 * st] = rd_sparseB(b, 256, w, lane); Wq[2 * st + 1] = rd_sparseB(b + 16384, 256, w, lane);
            Wk[2 * st] = rd_sparseB(b + 32768, 256, w, lane); Wk[2 * st + 1] = rd_sparseB(b + 49152, 256, w, lane);
            bar();
            if (st + 2 < 5) stage(st + 2);
        }
    }
    lds_t Mb = L;
    lds_t QT = L + 10240, KT = QT + 2048;
    lds_t XB = L + 28672;
    const unsigned a0 = afrag_off(lane);
    const int sidx = ((lane >> 3) & 1) ? 0xEEEE : 0x4444;
    const bool rowl = (lane & 15) < 8;
    const unsigned pxo = (w < 4 && rowl) ? px_base(0, w & 3, np) + ((h >> 1) >> 3) * 16384 + ((h >> 1) & 7) * 1024 + (h & 1) * 512 + (lane >> 4) * 128 + (lane & 7) * 16 : OOB;
    const unsigned pmo = (w < 4 && lane < 8) ? pm_base(0, w & 3, np) + (h >> 3) * 1024 + (h & 7) * 128 + lane * 16 : OOB;
    constexpr unsigned PXS = 8 * 2 * BLK, PMS = 2 * 2 * BLK;
    const int tro = (4 * (lane >> 4) + ((lane >> 2) & 3)) * 16 + (lane & 1) * 8;
    const unsigned xo = (unsigned)h * 8192u + lane * 16;
    auto dma_x = [&](int s) {
        lds_t xb = XB + (s & 1) * 16384;
#pragma unroll
        for (int k = 0; k < 4; ++k) { const int f = 4 * (w & 1) + k; dma16s(wsd, xb + (w & 2 ? 8192 : 0) + f * 1024, xo + f * 1024, ((w & 2) ? OFF_QX : OFF_KX) + (unsigned)s * (32u * 8192u)); }
    };
    stage_mem0(P, Mb);
    if (!wait_cnt(ws, C_CNTX, 32, 0x200)) return;
    if (w >= 4) dma_x(0);
    unsigned cx = 0;
    if (w >= 4) cx = ld32s(ws, C_CNTX + 128);
    constexpr float SC = 0.08838834764831845f * 1.4426950408889634f;
    unsigned nfail = 0;
    gstamp(P, h, 2);
    TS ts; ts.init();
    for (int s = 0; s < NSEG; ++s) {
        const bool on = s >= 8 && s < 60;
        if (!gather_m(P, ws, Mb, s, cls, w, lane, np, nfail)) return;
        ts.mark(0, on);
        drain();
        bar();
        if (s == 1) gstamp(P, h, 3);
        if (s == 63) gstamp(P, h, 5);
        if (SKEWST && w == 0 && s >= 30 && s < 38) skst(ws, lane, (h * 8 + (s - 30)) * 4 + 0);
        ts.mark(1, on);
        const bool last = s == NSEG - 1;
        lds_t xb = XB + (s & 1) * 16384;
        v4u af[10];
#pragma unroll
        for (int k = 0; k < 2; ++k) { af[k] = rd_afrag(Mb, a0, k); __builtin_amdgcn_sched_barrier(0); }
        f32x4 aq = {0.f, 0.f, 0.f, 0.f}, ak = {0.f, 0.f, 0.f, 0.f};
#pragma unroll
        for (int k = 0; k < 10; ++k) {
            if (k + 2 < 10) af[k + 2] = rd_afrag(Mb, a0, k + 2);
            __builtin_amdgcn_sched_barrier(0);
            aq = smf(af[k], Wq[k], aq, sidx); ak = smf(af[k], Wk[k], ak, sidx);
            if (k == 1) {
                const bool dop = s >= 1 && !last; const v4u ones = mk_ones();
                st128so(ws, dop ? pxo : OOB, (s & 1) ? 0u : PXS, ones); st128so(ws, dop ? pmo : OOB, (s & 1) ? 0u : PMS, ones);
            }
            __builtin_amdgcn_sched_barrier(0);
        }
        const bool crit = w < 4 && !last;
        v4u kx[4][2];
        if (crit) {
#pragma unroll
            for (int k = 0; k < 1; ++k) { kx[k][0] = *(LAS v4u*)(xb + (k * 2 + 0) * 1024 + lane * 16); kx[k][1] = *(LAS v4u*)(xb + (k * 2 + 1) * 1024 + lane * 16); }
        }
        const float v0 = fold2(aq[0], ak[0]), v1 = fold2(aq[1], ak[1]), v2 = fold2(aq[2], ak[2]), v3 = fold2(aq[3], ak[3]);
        *(LAS v2u*)(QT + (lane >> 5) * 2048 + (16 * w + (lane & 15)) * 16 + ((lane >> 4) & 1) * 8) = v2u{pk(v0, v1), pk(v2, v3)};
        ts.mark(2, on);
        bar();
        if (SKEWST && w == 0 && s >= 30 && s < 38) skst(ws, lane, (h * 8 + (s - 30)) * 4 + 2);
        ts.mark(3, on);
        if ((DEVDBG && (P.dbg & 1)) && s == 0) { ((v2u*)(P.ws + OFF_DBG + 6 * MB + h * 4096))[tid] = *(LAS v2u*)(QT + tid * 8); }
        if (w >= 4 && !last) {
            if (__builtin_amdgcn_readfirstlane(cx) + 1u < 32u) { if (!wait_cnt(ws, C_CNTX + (s + 1) * 128, 32, 0x200 + s)) return; }
            dma_x(s + 1);
            cx = ld32s(ws, s + 2 < NSEG ? C_CNTX + (s + 2) * 128 : OOB);
            if (h == 0 && w == 7) { int l2 = lane; asm volatile("" : "+v"(l2)); st32s(ws, l2 < 4 ? can_off(1, l2, np) : OOB, (unsigned)(s + 1)); }
        }
        if (crit) {
            __builtin_amdgcn_s_setprio(2);
            v4u qf[4], km[4];
            auto rdqk = [&](int k) { const v2u a = trd(QT + k * 512 + tro), b = trd(QT + k * 512 + tro + 256), c = trd(KT + k * 512 + tro), d = trd(KT + k * 512 + tro + 256);
                                     qf[k] = v4u{a.x, a.y, b.x, b.y}; km[k] = v4u{c.x, c.y, d.x, d.y}; };
            rdqk(0); __builtin_amdgcn_sched_barrier(0);
            kx[1][0] = *(LAS v4u*)(xb + 2 * 1024 + lane * 16); kx[1][1] = *(LAS v4u*)(xb + 3 * 1024 + lane * 16); rdqk(1);
            __builtin_amdgcn_sched_barrier(0);
            f32x4 t0 = {0.f, 0.f, 0.f, 0.f}, t1 = t0, tm = t0;
#pragma unroll
            for (int k = 0; k < 4; ++k) {
                if (k < 2) { rdqk(k + 2); kx[k + 2][0] = *(LAS v4u*)(xb + ((k + 2) * 2 + 0) * 1024 + lane * 16); kx[k + 2][1] = *(LAS v4u*)(xb + ((k + 2) * 2 + 1) * 1024 + lane * 16); }
                __builtin_amdgcn_sched_barrier(0);
                t0 = mf32(kx[k][0], qf[k], t0); t1 = mf32(kx[k][1], qf[k], t1); tm = mf32(km[k], qf[k], tm);
                __builtin_amdgcn_sched_barrier(0);
            }
            float x[4], m[4];
#pragma unroll
            for (int r = 0; r < 4; ++r) {
                x[r] = asf((unsigned)__builtin_amdgcn_update_dpp((int)asu(t0[r]), (int)asu(t1[r]), 0x118, 0xf, 0xc, false));
                m[r] = (rowl && lane < 32) ? tm[r] : -INFINITY;
            }
            float mx = vmax(vmax3(x[0], x[1], x[2]), vmax3(vmax3(x[3], m[0], m[1]), m[2], m[3]));
            mx = row_max3(mx);
            const float mc = mx * SC;
            float sum = 0.f;
#pragma unroll
            for (int r = 0; r < 4; ++r) { x[r] = __builtin_amdgcn_exp2f(__builtin_fmaf(x[r], SC, -mc)); m[r] = __builtin_amdgcn_exp2f(__builtin_fmaf(m[r], SC, -mc)); }
            sum = ((x[0] + x[1]) + (x[2] + x[3])) + ((m[0] + m[1]) + (m[2] + m[3]));
            sum += dppf<0x128>(sum); sum = swap32_sum(swap16_sum(sum));
            const float inv = __builtin_amdgcn_rcpf(sum);
            const unsigned xa = safe(pk(x[0] * inv, x[1] * inv)), xb2 = safe(pk(x[2] * inv, x[3] * inv)), ma = safe(pk(m[0] * inv, m[1] * inv)), mb = safe(pk(m[2] * inv, m[3] * inv));
            const unsigned ya = dppu<0x108>(xa), yb = dppu<0x108>(xb2);
            const auto ra = __builtin_amdgcn_permlane16_swap(ma, ma, false, false), rb = __builtin_amdgcn_permlane16_swap(mb, mb, false, false);
            drain();
            st128so(ws, pxo, (s & 1) ? PXS : 0u, v4u{xa, xb2, ya, yb});
            st128so(ws, pmo, (s & 1) ? PMS : 0u, v4u{ra[0], rb[0], ra[1], rb[1]});
            __builtin_amdgcn_s_setprio(0);
            if (SKEWST && w == 0 && s >= 30 && s < 38) skst(ws, lane, (h * 8 + (s - 30)) * 4 + 1);
            if ((DEVDBG && (P.dbg & 1)) && w == 0) {
                float* o = (float*)(P.ws + OFF_DBG + 2 * MB) + ((s * 32 + h) * 8 + (lane & 7)) * 48;
#pragma unroll
                for (int r = 0; r < 4; ++r) { o[16 + 16 * ((lane >> 3) & 1) + 4 * (lane >> 4) + r] = x[r] * inv; if (rowl && lane < 32) o[4 * (lane >> 4) + r] = m[r] * inv; }
            }
        }
        ts.mark(4, on);
        if (w == 4 || w == 5) {
            const int tq = w - 4;
            f32x4 tm = {0.f, 0.f, 0.f, 0.f}, t0 = tm, t1 = tm;
#pragma unroll
            for (int k = 0; k < 4; ++k) {
                const v4u qf = *(LAS v4u*)(xb + 8192 + (k * 2 + tq) * 1024 + lane * 16);
                const v4u k0 = *(LAS v4u*)(xb + (k * 2 + 0) * 1024 + lane * 16), k1 = *(LAS v4u*)(xb + (k * 2 + 1) * 1024 + lane * 16);
                const v2u ma = trd(KT + k * 512 + tro), mb = trd(KT + k * 512 + tro + 256);
                tm = mf32(v4u{ma.x, ma.y, mb.x, mb.y}, qf, tm); t0 = mf32(k0, qf, t0); t1 = mf32(k1, qf, t1);
            }
            const int ii = 16 * tq + (lane & 15), g = lane >> 4;
            float sv[12];
#pragma unroll
            for (int r = 0; r < 4; ++r) {
                sv[r] = g < 2 ? tm[r] : -INFINITY;
                sv[4 + r] = (4 * g + r <= ii) ? t0[r] : -INFINITY;
                sv[8 + r] = (16 + 4 * g + r <= ii) ? t1[r] : -INFINITY;
            }
            float mx = sv[0];
#pragma unroll
            for (int r = 1; r < 12; ++r) mx = fmaxf(mx, sv[r]);
            mx = swap32_max(swap16_max(mx));
            float sum = 0.f;
#pragma unroll
            for (int r = 0; r < 12; ++r) { sv[r] = __builtin_amdgcn_exp2f((sv[r] - mx) * SC); sum += sv[r]; }
            sum = swap32_sum(swap16_sum(sum));
            const float inv = __builtin_amdgcn_rcpf(sum);
            const auto r01 = __builtin_amdgcn_permlane16_swap(pk(sv[4] * inv, sv[5] * inv), pk(sv[8] * inv, sv[9] * inv), false, false);
            const auto r23 = __builtin_amdgcn_permlane16_swap(pk(sv[6] * inv, sv[7] * inv), pk(sv[10] * inv, sv[11] * inv), false, false);
            st128s(ws, OFF_PXX + (unsigned)((s * 32 + h) * 2 + tq) * 1024u + lane * 16, v4u{r01[0], r01[1], r23[0], r23[1]});
            st64s(ws, g < 2 ? OFF_PXM + (unsigned)(s * 32 + h) * 512u + ii * 16 + g * 8 : OOB, v2u{pk(sv[0] * inv, sv[1] * inv), pk(sv[2] * inv, sv[3] * inv)});
        }
        if (s >= 1 && tid == 448) cnt_add(P.ws, C_CNTP + (s - 1) * 128);
        ts.mark(5, on);
    }
    drain(); bar();
    if (tid == 0) cnt_add(P.ws, C_CNTP + (NSEG - 1) * 128);
    ts.dump(P, h, nfail);
}

__device__ __forceinline__ void role_vo(const Params& P, lds_t L, int h, int cls, int np) {
    const int tid = threadIdx.x, lane = tid & 63, w = __builtin_amdgcn_readfirstlane(tid >> 6);
    const rsrc_t ws = mkrs(P.ws, WS_USED);
    const v4u rv = mksrd(P.Wv, DM * 4096 * 2), ro = mksrd(P.Wo, 4096 * DM * 2);
    v8u Wv[10], Wo[10];
    {
        auto stage = [&](int st) { lds_t b = L + (st & 1) * 32768; stage_rows256<0>(rv, b, 128 * st, h, w, lane); stage_rows256<0>(rv, b + 16384, 128 * st + 64, h, w, lane); };
        stage(0); stage(1);
        stage_rows1280<0>(ro, L + 65536, 0, h, w, lane);
#pragma unroll
        for (int st = 0; st < 5; ++st) {
            drain();
            bar();
            lds_t b = L + (st & 1) * 32768;
            Wv[2 * st] = rd_sparseB(b, 256, w, lane); Wv[2 * st + 1] = rd_sparseB(b + 16384, 256, w, lane);
            if (st == 0) {
#pragma unroll
                for (int t = 0; t < 5; ++t) Wo[2 * t] = rd_sparseB(L + 65536, 1280, 5 * w + t, lane);
            }
            if (st == 1) {
#pragma unroll
                for (int t = 0; t < 5; ++t) Wo[2 * t + 1] = rd_sparseB(L + 65536, 1280, 5 * w + t, lane);
            }
            bar();
            if (st + 2 < 5) stage(st + 2);
            if (st == 0) stage_rows1280<0>(ro, L + 65536, 64, h, w, lane);
        }
    }
    lds_t Mb = L, VT = L + 10240;
    const unsigned a0 = afrag_off(lane);
    const int sidx = ((lane >> 3) & 1) ? 0xEEEE : 0x4444;
    const int tile4 = ((lane >> 4) & 1) * 2 + (lane >> 5);
    const unsigned zo4 = (unsigned)h * 256 + (lane & 15) * 16, zo1 = lane < 32 ? (unsigned)h * 256 + (lane & 15) * 16 + (lane >> 4) * 8 : OOB;
    const unsigned r4 = (5 * w + tile4) * 16384 + zo4, r1 = lane < 32 ? (5 * w + 4) * 16384 + zo1 : OOB;
    const unsigned q4 = (5 * w + tile4) * 8192 + zo4, q1 = lane < 32 ? (5 * w + 4) * 8192 + zo1 : OOB;
    const unsigned zb0 = zm_ring(0, 0, np), zb1 = zm_ring(1, 0, np);
    lds_t vtw = VT + (16 * w + ((lane & 15) ^ (4 * (w & 1)))) * 16 + ((lane >> 4) & 1) * 8;
    const v4u ones = {0xFFFFFFFFu, 0xFFFFFFFFu, 0xFFFFFFFFu, 0xFFFFFFFFu};
    stage_mem0(P, Mb);
    unsigned nfail = 0;
    const unsigned cano = (h == 0 && w == 0 && lane < 4) ? can_off(2, lane, np) : OOB;
    TS ts; ts.init();
    for (int s = 0; s < NSEG; ++s) {
        const bool on = s >= 8 && s < 60;
        if (!gather_m(P, ws, Mb, s, cls, w, lane, np, nfail)) return;
        ts.mark(0, on);
        drain();
        bar();
        ts.mark(1, on);
        const bool last = s == NSEG - 1;
        v4u af[10];
#pragma unroll
        for (int k = 0; k < 3; ++k) { af[k] = rd_afrag(Mb, a0, k); __builtin_amdgcn_sched_barrier(0); }
        f32x4 av = {0.f, 0.f, 0.f, 0.f}, av2 = av;
#pragma unroll
        for (int k = 0; k < 10; ++k) {
            if (k + 3 < 10) af[k + 3] = rd_afrag(Mb, a0, k + 3);
            __builtin_amdgcn_sched_barrier(0);
            if (k & 1) av2 = smf(af[k], Wv[k], av2, sidx); else av = smf(af[k], Wv[k], av, sidx);
            __builtin_amdgcn_sched_barrier(0);
        }
        av += av2;
        const float v0 = swap32_sum(av[0]), v1 = swap32_sum(av[1]), v2 = swap32_sum(av[2]), v3 = swap32_sum(av[3]);
        *(LAS v2u*)vtw = v2u{pk(v0, v1), pk(v2, v3)};
        if (VCAN == 0) st32s(ws, last ? OOB : cano, (unsigned)(s + 1));
        ts.mark(2, on);
        bar();
        if (VCAN == 1) st32s(ws, last ? OOB : cano, (unsigned)(s + 1));
        ts.mark(3, on);
        const v4u A0 = rd_afrag(VT, a0, 0);
        __builtin_amdgcn_sched_barrier(0);
        const v4u A1 = rd_afrag(VT, a0, 1);
        __builtin_amdgcn_sched_barrier(0);
        f32x4 az[5];
#pragma unroll
        for (int t = 0; t < 5; ++t) { az[t] = f32x4{0.f, 0.f, 0.f, 0.f}; az[t] = smf(A0, Wo[2 * t], az[t], sidx); }
        __builtin_amdgcn_sched_barrier(0);
#pragma unroll
        for (int t = 0; t < 5; ++t) az[t] = smf(A1, Wo[2 * t + 1], az[t], sidx);
        if (VCAN == 2) st32s(ws, last ? OOB : cano, (unsigned)(s + 1));
        if (SKEWST && w == 7 && s >= 30 && s < 38) skst(ws, lane, ((32 + h) * 8 + (s - 30)) * 4 + 1);
        const unsigned a1 = safe(pk(fold2(az[0][0], az[1][0]), fold2(az[0][1], az[1][1]))), b1 = safe(pk(fold2(az[0][2], az[1][2]), fold2(az[0][3], az[1][3])));
        const unsigned a2 = safe(pk(fold2(az[2][0], az[3][0]), fold2(az[2][1], az[3][1]))), b2 = safe(pk(fold2(az[2][2], az[3][2]), fold2(az[2][3], az[3][3])));
        const auto ra = __builtin_amdgcn_permlane16_swap(a1, a2, false, false);
        const auto rb = __builtin_amdgcn_permlane16_swap(b1, b2, false, false);
        const v4u o4 = {ra[0], rb[0], ra[1], rb[1]};
        const v2u o1 = {safe(pk(swap32_sum(az[4][0]), swap32_sum(az[4][1]))), safe(pk(swap32_sum(az[4][2]), swap32_sum(az[4][3])))};
        ts.mark(4, on);
        {
            const unsigned zb = (s & 1) ? zb1 : zb0;
            st128so(ws, !last ? r4 : OOB, zb, o4);
            st64so(ws, !last ? r1 : OOB, zb, o1);
            st128so(ws, q4, OFF_ZMP + (unsigned)s * (40u * 8192u), o4);
            st64so(ws, q1, OFF_ZMP + (unsigned)s * (40u * 8192u), o1);
        }
        if (s >= 1 && tid == 0) cnt_add(P.ws, C_CNTZM + (s - 1) * 128);
        ts.mark(6, on);
    }
    drain(); bar();
    if (tid == 0) cnt_add(P.ws, C_CNTZM + (NSEG - 1) * 128);
    ts.dump(P, 32 + h, nfail);
}

__device__ __forceinline__ v2u rd2(lds_t p, int o0, int o1) { v2u r; asm volatile("ds_read2_b32 %0, %1 offset0:%2 offset1:%3" : "=v"(r) : "v"((unsigned)(unsigned long long)p), "i"(o0), "i"(o1) : "memory"); return r; }
__device__ __forceinline__ void role_red(const Params& P, lds_t L, int c, int cls, int np) {
    const int tid = threadIdx.x, lane = tid & 63, w = __builtin_amdgcn_readfirstlane(tid >> 6);
    const rsrc_t ws = mkrs(P.ws, WS_USED);
    lds_t ZB = L;
    lds_t ZR = L + 65536;
    volatile LAS int* zflag = (volatile LAS int*)(L + 65536 + 8192);
    const unsigned lo = lane * 16;
    if (tid == 0) { zflag[0] = 0; zflag[1] = 0; }
    if (w != 0) {
        const v4u wsd = mksrd(P.ws, WS_USED);
        unsigned nfz = 0;
        for (int s = 0; s < NSEG - 1; ++s) {
            if (w >= 4) {
                if (!wait_cnt(ws, C_CNTZ + s * 128, 32, 0x300 + s)) return;
#pragma unroll
                for (int k = 0; k < 8; ++k) dma16s(wsd, ZB + (s & 1) * 32768 + (8 * (w - 4) + k) * 1024, lo, OFF_ZX + (unsigned)(s * 40 + c) * 32768u + (8 * (w - 4) + k) * 1024);
                drain();
            }
            bar();
            if (w == 1) {
                const unsigned zbase = zm_ring(s & 1, c, np);
                bool dead = !canary_wait(ws, can_off(2, cls, np), s + 1, P.dz, 0x380 + s);
                if (SKEWST && s >= 30 && s < 38) skst(ws, lane, ((64 + c) * 8 + (s - 30)) * 4 + 2);
                int redo = 0;
                unsigned spins = 0;
                while (!dead) {
#pragma unroll
                    for (int k = 0; k < 8; ++k) dma16s(wsd, ZR + k * 1024, lo, zbase + k * 1024);
                    drain();
                    if (!redo) zflag[0] = s + 1;
                    if (SKEWST && !redo && s >= 30 && s < 38) skst(ws, lane, ((64 + c) * 8 + (s - 30)) * 4 + 3);
                    unsigned mz = 0;
#pragma unroll
                    for (int k = 0; k < 8; ++k) mz = umax4(mz, *(LAS v4u*)(ZR + k * 1024 + lo));
                    if (__all(mz != 0xFFFFFFFFu)) break;
                    redo = 1 << 16;
                    for (int z = 0; z < P.retry; ++z) __builtin_amdgcn_s_sleep(1);
                    dead = !spin_more(ws, spins, 0x400 + s);
                }
                if (dead) { zflag[0] = 1 << 20; zflag[1] = 1 << 20; return; }
                zflag[1] = (s + 1) | redo;
                nfz += redo ? 1 : 0;
                const v4u ones = mk_ones();
#pragma unroll
                for (int k = 0; k < 8; ++k) st128so(ws, lo + (k & 3) * 1024, zbase + (k >> 2) * 4096, ones);
                drain();
            }
        }
        if ((STAMPS || STAMPLITE) && w == 1 && lane == 0) ((unsigned*)(P.ws + OFF_DBG + 8 * MB))[((64 + c) * 8 + 1) * 16 + 14] = nfz;
        return;
    }
    const int sidx = ((lane >> 3) & 1) ? 0xEEEE : 0x4444;
    const unsigned mo = (unsigned)(lane >> 4) * (2 * 16384) + fragoff(c >> 2) + (c & 3) * 256 + ((lane & 15) ^ (4 * (c & 1))) * 16;
    const unsigned cano = (c == 0 && lane < 4) ? can_off(0, lane, np) : OOB;
    lds_t zr = ZR + (lane >> 5) * 512 + (lane & 15) * 16 + ((lane >> 4) & 1) * 8;
    const unsigned mb0 = m_base(0, 0, np);
    unsigned nfail = 0;
    TS ts; ts.init();
    TS tr; tr.initR();
    int slot_new = 1, slot_old = 0;
    for (int s = 0; s < NSEG - 1; ++s) {
        const bool on = s >= 8 && s < 60;
        tr.markR(0, on);
        bar();
        tr.markR(1, on);
        const int sl = s & 1;
        lds_t zb = ZB + sl * 32768 + lane * 16;
        const unsigned pxb = px_base(sl, cls, np), pmb = pm_base(sl, cls, np);
        const unsigned mnew = mb0 + slot_new * (8 * 2 * BLK), mold = mb0 + slot_old * (8 * 2 * BLK);
        auto attempt = [&](bool first, bool& ok) -> v4u {
            v4u B[32];
#pragma unroll
            for (int k = 0; k < 32; ++k) B[k] = *(LAS v4u*)(zb + k * 1024);
            if (first) ok = canary_wait(ws, can_off(1, cls, np), s + 1, P.dr, 0x480 + s);
            asm volatile("" : "+v"(B[0]), "+v"(B[31]));
            tr.markR(2, on && first);
            if (SKEWST && first && s >= 30 && s < 38) skst(ws, lane, ((64 + c) * 8 + (s - 30)) * 4 + 0);
            v4u p[20];
#pragma unroll
            for (int f = 0; f < 16; ++f) { p[f] = ld128so(ws, lo + (f & 3) * 1024, pxb + (f >> 3) * 16384 + ((f >> 2) & 1) * 4096); __builtin_amdgcn_sched_barrier(0); }
#pragma unroll
            for (int f = 0; f < 4; ++f) { p[16 + f] = ld128so(ws, lo + f * 1024, pmb); __builtin_amdgcn_sched_barrier(0); }
            tr.markR(3, on && first);
            f32x4 acc0 = {0.f, 0.f, 0.f, 0.f}, acc1 = acc0;
            unsigned mpa = 0, mpb = 0;
#pragma unroll
            for (int f = 0; f < 10; ++f) {
                const v4u x = B[2 * f], y = B[2 * f + 1];
                if (f & 1) acc1 = smf(p[f], v8u{x.x, x.y, x.z, x.w, y.x, y.y, y.z, y.w}, acc1, sidx); else acc0 = smf(p[f], v8u{x.x, x.y, x.z, x.w, y.x, y.y, y.z, y.w}, acc0, sidx);
                mpa = umax3(mpa, p[f].x, p[f].y); mpb = umax3(mpb, p[f].z, p[f].w); asm volatile("" : "+v"(mpa), "+v"(mpb));
                if (first && f == RCAN) st32s(ws, cano, (unsigned)(s + 1));
                if (RSTAMPS && f == 0) { asm volatile("" :: "v"(acc0)); tr.markR(4, on && first); }
                __builtin_amdgcn_sched_barrier(0);
            }
            if (RSTAMPS) { asm volatile("" :: "v"(acc1)); tr.markR(5, on && first); }
            if (first) { while (zflag[0] < s + 1) {} }
            tr.markR(6, on && first);
            v2u z[4][4];
#pragma unroll
            for (int F = 0; F < 4; ++F) { z[F][0] = rd2(zr + F * 2048, 0, 64); z[F][1] = rd2(zr + F * 2048, 1, 65); z[F][2] = rd2(zr + F * 2048 + 1024, 0, 64); z[F][3] = rd2(zr + F * 2048 + 1024, 1, 65); }
            __builtin_amdgcn_sched_barrier(0);
#pragma unroll
            for (int f = 10; f < 16; ++f) {
                const v4u x = B[2 * f], y = B[2 * f + 1];
                if (f & 1) acc1 = smf(p[f], v8u{x.x, x.y, x.z, x.w, y.x, y.y, y.z, y.w}, acc1, sidx); else acc0 = smf(p[f], v8u{x.x, x.y, x.z, x.w, y.x, y.y, y.z, y.w}, acc0, sidx);
                mpa = umax3(mpa, p[f].x, p[f].y); mpb = umax3(mpb, p[f].z, p[f].w); asm volatile("" : "+v"(mpa), "+v"(mpb));
                if (first && f == RCAN) st32s(ws, cano, (unsigned)(s + 1));
                __builtin_amdgcn_sched_barrier(0);
            }
            asm volatile("s_waitcnt lgkmcnt(0)" : "+v"(z[0][0]), "+v"(z[0][1]), "+v"(z[0][2]), "+v"(z[0][3]), "+v"(z[1][0]), "+v"(z[1][1]), "+v"(z[1][2]), "+v"(z[1][3]),
                         "+v"(z[2][0]), "+v"(z[2][1]), "+v"(z[2][2]), "+v"(z[2][3]), "+v"(z[3][0]), "+v"(z[3][1]), "+v"(z[3][2]), "+v"(z[3][3]) :: "memory");
            __builtin_amdgcn_sched_barrier(0);
#pragma unroll
            for (int F = 0; F < 4; ++F) {
                const v8u bz = {z[F][0].x, z[F][0].y, z[F][1].x, z[F][1].y, z[F][2].x, z[F][2].y, z[F][3].x, z[F][3].y};
                if (F & 1) acc1 = smf(p[16 + F], bz, acc1, sidx); else acc0 = smf(p[16 + F], bz, acc0, sidx);
                mpa = umax3(mpa, p[16 + F].x, p[16 + F].y); mpb = umax3(mpb, p[16 + F].z, p[16 + F].w); asm volatile("" : "+v"(mpa), "+v"(mpb));
                if (first && 16 + F == RCAN) st32s(ws, cano, (unsigned)(s + 1));
                __builtin_amdgcn_sched_barrier(0);
            }
            const unsigned mp = max(mpa, mpb);
            acc0 += acc1;
            const unsigned d0 = safe(pk(swap32_sum(acc0[0]), swap32_sum(acc0[1]))), d1 = safe(pk(swap32_sum(acc0[2]), swap32_sum(acc0[3])));
            const auto r0 = __builtin_amdgcn_permlane16_swap(d0, d0, false, false);
            const auto r1 = __builtin_amdgcn_permlane16_swap(d1, d1, false, false);
            const int zv = zflag[1];
            ok = ok && __all(mp != 0xFFFFFFFFu);
            int zz = zv; while (zz < s + 1 || ((zz & 0xFFFF) < s + 1 && zz < (1 << 20))) zz = zflag[1];
            if (first) ok = ok && zz == s + 1;
            return v4u{r0[0], r1[0], r0[1], r1[1]};
        };
        bool ok = true;
        v4u o = attempt(true, ok);
        st128so(ws, ok ? mo : OOB, mnew, o);
        tr.markR(8, on);
        if (SKEWST && s >= 30 && s < 38) skst(ws, lane, ((64 + c) * 8 + (s - 30)) * 4 + 1);
        if (!ok) {
            ++nfail;
            unsigned spins = 0;
            for (;;) {
                for (int z = 0; z < P.retry; ++z) __builtin_amdgcn_s_sleep(1);
                if (!spin_more(ws, spins, 0x500 + s)) return;
                ok = true;
                o = attempt(false, ok);
                if (ok) break;
            }
            st128so(ws, mo, mnew, o);
            st32s(ws, cano, (unsigned)(s + 1));
        }
        if (DEVDBG && (P.dbg & 1)) st128s(ws, lane < 16 ? OFF_DBG + (unsigned)(s + 1) * 10240u + (unsigned)c * 256 + lane * 16 : OOB, o);
        st128so(ws, s >= 1 ? mo : OOB, mold, mk_ones());
        slot_old = slot_new; slot_new = slot_new == 2 ? 0 : slot_new + 1;
    }
    ts.dump(P, 64 + c, nfail);
    tr.dumpR(P, c);
}

__device__ __forceinline__ void stage_x(v4u rx, lds_t dst, int s, const unsigned (&xo)[5], int w) {
#pragma unroll
    for (int m = 0; m < 5; ++m) dma16(rx, dst + (w + 8 * m) * 1024, xo[m] + (unsigned)s * (32u * 1280u));
}
__device__ __forceinline__ void init_xo(unsigned (&xo)[5], int w, int lane) {
#pragma unroll
    for (int m = 0; m < 5; ++m) {
        const int S = (w + 8 * m) * 64 + lane, tok = S / 80, sp = S % 80;
        const int src = (sp & ~15) | ((sp & 15) ^ (tok & 15));
        xo[m] = (unsigned)(tok * 1280 + src * 16);
    }
}
__device__ __forceinline__ void role_xqk(const Params& P, lds_t L, int h) {
    const int tid = threadIdx.x, lane = tid & 63, w = __builtin_amdgcn_readfirstlane(tid >> 6);
    const rsrc_t ws = mkrs(P.ws, WS_USED);
    const v4u rq = mksrd(P.Wq, DM * 4096 * 2), rk = mksrd(P.Wk, DM * 4096 * 2), rx = mksrd(P.x, SEQ * DM * 2);
    v4u W[2][20];
    {
        auto stage = [&](int st) {
            lds_t b = L + (st & 1) * 65536;
            stage_rows256<1>(rq, b, 128 * st, h, w, lane); stage_rows256<1>(rq, b + 16384, 128 * st + 64, h, w, lane);
            stage_rows256<1>(rk, b + 32768, 128 * st, h, w, lane); stage_rows256<1>(rk, b + 49152, 128 * st + 64, h, w, lane);
        };
        stage(0); stage(1);
#pragma unroll
        for (int st = 0; st < 5; ++st) {
            if (st < 4) asm volatile("s_waitcnt vmcnt(8)" ::: "memory"); else drain();
            bar();
            lds_t b = L + (st & 1) * 65536 + (w >> 2) * 32768;
#pragma unroll
            for (int k = 0; k < 4; ++k)
#pragma unroll
                for (int mt = 0; mt < 2; ++mt) W[mt][4 * st + k] = rd_denseNat(b + (k >> 1) * 16384, 256, 32 * (k & 1), 2 * (w & 3) + mt, lane);
            bar();
            if (st + 2 < 5) stage(st + 2);
        }
    }
    unsigned xo[5]; init_xo(xo, w, lane);
    const int XT = 40960;
    stage_x(rx, L, 0, xo, w);
    unsigned bo[4];
#pragma unroll
    for (int k = 0; k < 4; ++k) bo[k] = (lane & 15) * 1280 + (((4 * k + (lane >> 4)) ^ (lane & 15)) * 16);
    const unsigned obase = (w < 4 ? OFF_QX : OFF_KX) + (w & 3) * 2048 + lane * 16;
    gstamp(P, 128 + h, 2);
    for (int s = 0; s < NSEG; ++s) {
        if (s >= 3) asm volatile("s_waitcnt vmcnt(2)" ::: "memory"); else drain();
        bar();
        if (tid == 0) { if (s >= 4) cnt_add(P.ws, C_CNTX + (s - 2) * 128); else if (s == 1 || s == 2) cnt_add(P.ws, C_CNTX + (s - 1) * 128); }
        if (s == 1) gstamp(P, 128 + h, 3);
        if (s + 1 < NSEG) stage_x(rx, L + ((s + 1) & 1) * XT, s + 1, xo, w);
        lds_t xb = L + (s & 1) * XT;
        f32x4 acc[2][2];
#pragma unroll
        for (int a = 0; a < 2; ++a)
#pragma unroll
            for (int b = 0; b < 2; ++b) acc[a][b] = f32x4{0.f, 0.f, 0.f, 0.f};
        v4u bf[20][2];
#pragma unroll
        for (int k = 0; k < 3; ++k)
#pragma unroll
            for (int t = 0; t < 2; ++t) bf[k][t] = *(LAS v4u*)(xb + t * 16 * 1280 + (k >> 2) * 256 + bo[k & 3]);
#pragma unroll
        for (int k = 0; k < 20; ++k) {
            if (k + 3 < 20) {
#pragma unroll
                for (int t = 0; t < 2; ++t) bf[k + 3][t] = *(LAS v4u*)(xb + t * 16 * 1280 + ((k + 3) >> 2) * 256 + bo[(k + 3) & 3]);
            }
            __builtin_amdgcn_sched_barrier(0);
#pragma unroll
            for (int t = 0; t < 2; ++t) { acc[0][t] = mf32(W[0][k], bf[k][t], acc[0][t]); acc[1][t] = mf32(W[1][k], bf[k][t], acc[1][t]); }
            __builtin_amdgcn_sched_barrier(0);
        }
#pragma unroll
        for (int t = 0; t < 2; ++t)
            st128s(ws, obase + (unsigned)(s * 32 + h) * 8192u + t * 1024, v4u{pk(acc[0][t][0], acc[0][t][1]), pk(acc[0][t][2], acc[0][t][3]), pk(acc[1][t][0], acc[1][t][1]), pk(acc[1][t][2], acc[1][t][3])});
    }
    drain(); bar();
    if (tid == 0) { cnt_add(P.ws, C_CNTX + (NSEG - 2) * 128); cnt_add(P.ws, C_CNTX + (NSEG - 1) * 128); }
}
__device__ __forceinline__ void role_xv(const Params& P, lds_t L, int h) {
    const int tid = threadIdx.x, lane = tid & 63, w = __builtin_amdgcn_readfirstlane(tid >> 6);
    const rsrc_t ws = mkrs(P.ws, WS_USED);
    const v4u rv = mksrd(P.Wv, DM * 4096 * 2), ro = mksrd(P.Wo, 4096 * DM * 2), rx = mksrd(P.x, SEQ * DM * 2);
    v4u Wv[20], Wo[5][4];
    {
        auto stage = [&](int st) { lds_t b = L + (st & 1) * 32768; stage_rows256<1>(rv, b, 128 * st, h, w, lane); stage_rows256<1>(rv, b + 16384, 128 * st + 64, h, w, lane); };
        stage(0); stage(1);
        stage_rows1280<2>(ro, L + 65536, 0, h, w, lane);
#pragma unroll
        for (int st = 0; st < 5; ++st) {
            drain();
            bar();
            lds_t b = L + (st & 1) * 32768;
#pragma unroll
            for (int k = 0; k < 4; ++k) Wv[4 * st + k] = rd_denseNat(b + (k >> 1) * 16384, 256, 32 * (k & 1), w, lane);
            if (st < 2) {
#pragma unroll
                for (int t = 0; t < 5; ++t)
#pragma unroll
                    for (int k = 0; k < 2; ++k) Wo[t][2 * st + k] = rd_densePerm(L + 65536, 1280, 32 * k, 5 * w + t, lane);
            }
            bar();
            if (st + 2 < 5) stage(st + 2);
            if (st == 0) stage_rows1280<2>(ro, L + 65536, 64, h, w, lane);
        }
    }
    unsigned xo[5]; init_xo(xo, w, lane);
    const int XT = 40960;
    lds_t VB = L + 2 * XT;
    gstamp(P, 160 + h, 2);
    stage_x(rx, L, 0, xo, w);
    unsigned bo[4];
#pragma unroll
    for (int k = 0; k < 4; ++k) bo[k] = (lane & 15) * 1280 + (((4 * k + (lane >> 4)) ^ (lane & 15)) * 16);
    for (int s = 0; s < NSEG; ++s) {
        if (s >= 3) asm volatile("s_waitcnt vmcnt(5)" ::: "memory"); else drain();
        bar();
        if (tid == 0) { if (s >= 4) cnt_add(P.ws, C_CNTZ + (s - 2) * 128); else if (s == 1 || s == 2) cnt_add(P.ws, C_CNTZ + (s - 1) * 128); }
        if (s == 1) gstamp(P, 160 + h, 3);
        if (s + 1 < NSEG) stage_x(rx, L + ((s + 1) & 1) * XT, s + 1, xo, w);
        lds_t xb = L + (s & 1) * XT;
        f32x4 av[2] = {f32x4{0.f, 0.f, 0.f, 0.f}, f32x4{0.f, 0.f, 0.f, 0.f}};
        v4u bf[20][2];
#pragma unroll
        for (int k = 0; k < 4; ++k)
#pragma unroll
            for (int t = 0; t < 2; ++t) bf[k][t] = *(LAS v4u*)(xb + t * 16 * 1280 + (k >> 2) * 256 + bo[k & 3]);
#pragma unroll
        for (int k = 0; k < 20; ++k) {
            if (k + 4 < 20) {
#pragma unroll
                for (int t = 0; t < 2; ++t) bf[k + 4][t] = *(LAS v4u*)(xb + t * 16 * 1280 + ((k + 4) >> 2) * 256 + bo[(k + 4) & 3]);
            }
            __builtin_amdgcn_sched_barrier(0);
#pragma unroll
            for (int t = 0; t < 2; ++t) av[t] = mf32(Wv[k], bf[k][t], av[t]);
            __builtin_amdgcn_sched_barrier(0);
        }
#pragma unroll
        for (int t = 0; t < 2; ++t) *(LAS v2u*)(VB + (t * 4 + (w >> 1)) * 1024 + lane * 16 + 8 * (w & 1)) = v2u{pk(av[t][0], av[t][1]), pk(av[t][2], av[t][3])};
        bar();
        v4u va[2][4];
#pragma unroll
        for (int k = 0; k < 4; ++k)
#pragma unroll
            for (int t = 0; t < 2; ++t) va[t][k] = *(LAS v4u*)(VB + (t * 4 + k) * 1024 + lane * 16);
        f32x4 az[2][5];
#pragma unroll
        for (int t = 0; t < 2; ++t)
#pragma unroll
            for (int c = 0; c < 5; ++c) az[t][c] = f32x4{0.f, 0.f, 0.f, 0.f};
#pragma unroll
        for (int k = 0; k < 4; ++k)
#pragma unroll
            for (int t = 0; t < 2; ++t)
#pragma unroll
                for (int c = 0; c < 5; ++c) az[t][c] = mf32(va[t][k], Wo[c][k], az[t][c]);
#pragma unroll
        for (int c = 0; c < 5; ++c) {
            const auto r01 = __builtin_amdgcn_permlane16_swap(pk(az[0][c][0], az[0][c][1]), pk(az[1][c][0], az[1][c][1]), false, false);
            const auto r23 = __builtin_amdgcn_permlane16_swap(pk(az[0][c][2], az[0][c][3]), pk(az[1][c][2], az[1][c][3]), false, false);
            st128s(ws, OFF_ZX + (unsigned)(s * 40 + 5 * w + c) * 32768u + h * 1024 + lane * 16, v4u{r01[0], r01[1], r23[0], r23[1]});
        }
    }
    drain(); bar();
    if (tid == 0) { cnt_add(P.ws, C_CNTZ + (NSEG - 2) * 128); cnt_add(P.ws, C_CNTZ + (NSEG - 1) * 128); }
}

__device__ __forceinline__ float bf2f(unsigned short b) { return __builtin_bit_cast(float, (unsigned)b << 16); }
__device__ __forceinline__ float bfround(float f) { return asf(pk(f, 0.f) << 16); }
__device__ __forceinline__ f32x4 mff32(float a, float b, f32x4 c) { return __builtin_amdgcn_mfma_f32_16x16x4f32(a, b, c, 0, 0, 0); }

__device__ __forceinline__ bool final_job(const Params& P, rsrc_t ws, lds_t L, int s, int cg) {
    const int tid = threadIdx.x, lane = tid & 63, w = __builtin_amdgcn_readfirstlane(tid >> 6);
    if (!wait_cnt(ws, C_CNTP + s * 128, 32, 0x700 + s)) return false;
    if (!wait_cnt(ws, C_CNTZM + s * 128, 32, 0x780 + s)) return false;
    if (!wait_cnt(ws, C_CNTZ + s * 128, 32, 0x7C0 + s)) return false;
    const unsigned lo = lane * 16;
    v4u ax[4][2], am[2], bx[4][4], bm[4];
#pragma unroll
    for (int hh = 0; hh < 4; ++hh) {
#pragma unroll
        for (int t = 0; t < 2; ++t) ax[hh][t] = ld128so(ws, lo + t * 1024, OFF_PXX + (unsigned)(s * 32 + 4 * w + hh) * 2048u);
#pragma unroll
        for (int c = 0; c < 4; ++c) bx[hh][c] = ld128so(ws, lo, OFF_ZX + (unsigned)(s * 40 + 4 * cg + c) * 32768u + (4 * w + hh) * 1024);
    }
#pragma unroll
    for (int t = 0; t < 2; ++t) am[t] = ld128so(ws, (lane >> 4) * 512 + (16 * t + (lane & 15)) * 16, OFF_PXM + (unsigned)(s * 32 + 4 * w) * 512u);
#pragma unroll
    for (int c = 0; c < 4; ++c) bm[c] = ld128so(ws, lo, OFF_ZMP + (unsigned)(s * 40 + 4 * cg + c) * 8192u + 4 * w * 256);
    f32x4 acc[2][4];
#pragma unroll
    for (int t = 0; t < 2; ++t)
#pragma unroll
        for (int c = 0; c < 4; ++c) acc[t][c] = f32x4{0.f, 0.f, 0.f, 0.f};
#pragma unroll
    for (int hh = 0; hh < 4; ++hh)
#pragma unroll
        for (int t = 0; t < 2; ++t)
#pragma unroll
            for (int c = 0; c < 4; ++c) acc[t][c] = mf32(ax[hh][t], bx[hh][c], acc[t][c]);
#pragma unroll
    for (int t = 0; t < 2; ++t)
#pragma unroll
        for (int c = 0; c < 4; ++c) acc[t][c] = mf32(am[t], bm[c], acc[t][c]);
    bar();
#pragma unroll
    for (int t = 0; t < 2; ++t)
#pragma unroll
        for (int c = 0; c < 4; ++c) *(LAS f32x4*)(L + ((w * 2 + t) * 4 + c) * 1024 + lo) = acc[t][c];
    bar();
    f32x4 v = {0.f, 0.f, 0.f, 0.f};
#pragma unroll
    for (int ww = 0; ww < 8; ++ww) v += *(LAS f32x4*)(L + (ww * 8 + w) * 1024 + lo);
    lds_t OT = L + 65536;
    {
        const int tok = 16 * (w >> 2) + 4 * (lane >> 4), col = 16 * (w & 3) + (lane & 15);
#pragma unroll
        for (int r = 0; r < 4; ++r) *(LAS unsigned short*)(OT + (tok + r) * 144 + col * 2) = (unsigned short)(pk(v[r], 0.f) & 0xffffu);
    }
    bar();
    if (tid < 256) {
        const int row = tid >> 3, ch = tid & 7;
        const v4u o = *(LAS v4u*)(OT + row * 144 + ch * 16);
        *(v4u*)((char*)P.out + (size_t)(32 * s + row) * 2560 + 1280 + cg * 128 + ch * 16) = o;
    }
    return true;
}

__device__ __forceinline__ void ssm_job(const Params& P, lds_t L, int d0) {
    const int tid = threadIdx.x, lane = tid & 63, w = __builtin_amdgcn_readfirstlane(tid >> 6);
    constexpr int UPAD = 256, UST = (UPAD + 2048) * 2 + 32;
    lds_t UT = L;
    lds_t KD = L + 37376;
    lds_t PR = L + 58368;
    lds_t RV = L + 74752;
    lds_t TC = L + 79104;
    constexpr int TST = 4144;
    bar();
    for (int i = tid; i < 8 * (UPAD / 2); i += 512) *(LAS unsigned*)(UT + (i / (UPAD / 2)) * UST + (i % (UPAD / 2)) * 4) = 0u;
#pragma unroll
    for (int it = 0; it < 4; ++it) {
        const int l = tid + 512 * it;
        const v4u v = *(const v4u*)((const char*)P.u + (size_t)l * 1280 + d0 * 2);
        const unsigned e[4] = {v.x, v.y, v.z, v.w};
#pragma unroll
        for (int c = 0; c < 8; ++c) *(LAS unsigned short*)(UT + c * UST + (UPAD + l) * 2) = (unsigned short)((c & 1) ? e[c >> 1] >> 16 : e[c >> 1] & 0xffffu);
    }
    const int d = d0 + w;
    {
        const float dt = bfround(expf(bf2f(((const unsigned short*)P.logdt)[d])));
#pragma unroll
        for (int it = 0; it < 2; ++it) {
            const int n = lane + 64 * it, o = d * 128 + n;
            const float ar = dt * bf2f(((const unsigned short*)P.Are)[o]), ai = dt * bf2f(((const unsigned short*)P.Aim)[o]);
            const float br = dt * bf2f(((const unsigned short*)P.Bre)[o]), bi = dt * bf2f(((const unsigned short*)P.Bim)[o]);
            const float cr = bf2f(((const unsigned short*)P.Cre)[o]), ci = bf2f(((const unsigned short*)P.Cim)[o]);
            *(LAS f32x4*)(PR + (w * 128 + n) * 16) = f32x4{ar * 1.4426950408889634f, ai * 0.15915494309189535f, cr * br - ci * bi, cr * bi + ci * br};
        }
    }
    bar();
#pragma unroll 1
    for (int r = 0; r < 4; ++r) {
        const int step = 1 << r, base = r == 0 ? 0 : 128 << r;
        const float la = (float)(base + 16 * step * (lane & 15)), lb = (float)(step * (lane & 15));
        f32x4 a0 = {0.f, 0.f, 0.f, 0.f}, a1 = a0;
#pragma unroll 2
        for (int i = 0; i < 32; ++i) {
            const f32x4 q = *(LAS f32x4*)(PR + (w * 128 + 4 * i + (lane >> 4)) * 16);
            const float ea = __builtin_amdgcn_exp2f(q[0] * la), eb = __builtin_amdgcn_exp2f(q[0] * lb);
            const float pa = __builtin_amdgcn_fractf(q[1] * la), pb = __builtin_amdgcn_fractf(q[1] * lb);
            const float ca = ea * __builtin_amdgcn_cosf(pa), sa = ea * __builtin_amdgcn_sinf(pa);
            const float er = eb * __builtin_amdgcn_cosf(pb), ei = eb * __builtin_amdgcn_sinf(pb);
            const float fr = q[2] * ca - q[3] * sa, fi = q[2] * sa + q[3] * ca;
            a0 = mff32(fr, er, a0); a1 = mff32(-fi, ei, a1);
        }
        a0 += a1;
        const int kb = r == 0 ? 0 : 128 + 128 * r;
#pragma unroll
        for (int rr = 0; rr < 4; ++rr) {
            const int j = 16 * (4 * (lane >> 4) + rr) + (lane & 15);
            if (r == 0 || j < 128 || (r == 3 && j == 128)) *(LAS float*)(KD + (w * 648 + kb + j) * 4) = a0[rr];
        }
    }
    bar();
    const int tile = w < 4 ? w : 11 - w;
    const int b = lane & 15, g = lane >> 4, sg = (8 - (b & 7)) & 7;
    lds_t ap = TC + sg * TST + (2048 - 16 * (g >> 1) - (b + sg) + 8 * (g & 1)) * 2;
    const float dsk_unused = 0.f; (void)dsk_unused;
#pragma unroll 1
    for (int c = 0; c < 8; ++c) {
        for (int x = tid; x < 2080; x += 512) {
            const int m = 2048 - x;
            float kv = 0.f;
            if (m >= 0 && m < 2048) {
                const int lt = (int)bfround((float)m);
                const int ix = lt < 256 ? lt : lt < 512 ? 256 + ((lt - 256) >> 1) : lt < 1024 ? 384 + ((lt - 512) >> 2) : 512 + ((lt - 1024) >> 3);
                kv = *(LAS float*)(KD + (c * 648 + ix) * 4);
            }
            *(LAS unsigned short*)(RV + x * 2) = (unsigned short)(pk(kv, 0.f) & 0xffffu);
        }
        bar();
        if (tid < 258) {
            const v4u p0 = *(LAS v4u*)(RV + tid * 16), p1 = *(LAS v4u*)(RV + tid * 16 + 16);
            const unsigned e[8] = {p0.x, p0.y, p0.z, p0.w, p1.x, p1.y, p1.z, p1.w};
#pragma unroll
            for (int sgm = 0; sgm < 8; ++sgm) {
                v4u o;
#pragma unroll
                for (int k = 0; k < 4; ++k) o[k] = (sgm & 1) ? __builtin_amdgcn_alignbit(e[(sgm >> 1) + k + 1], e[(sgm >> 1) + k], 16) : e[(sgm >> 1) + k];
                *(LAS v4u*)(TC + sgm * TST + tid * 16) = o;
            }
        }
        bar();
        lds_t bp = UT + c * UST + (UPAD + 256 * tile + 16 * b - 16 * (g >> 1) + 8 * (g & 1)) * 2;
        f32x4 y0 = {0.f, 0.f, 0.f, 0.f}, y1 = y0;
        const int ne = 8 * tile + 8;
#pragma unroll 4
        for (int e = 0; e < ne; e += 2) {
            y0 = mf32(*(LAS v4u*)(ap - 64 * e), *(LAS v4u*)(bp - 64 * e), y0);
            y1 = mf32(*(LAS v4u*)(ap - 64 * e - 64), *(LAS v4u*)(bp - 64 * e - 64), y1);
        }
        y0 += y1;
        const int l0 = 16 * (16 * tile + b) + 4 * g;
        lds_t up = UT + c * UST + (UPAD + l0) * 2;
        const v2u uu = *(LAS v2u*)up;
        const float dsk = bf2f(((const unsigned short*)P.Dsk)[d0 + c]);
        const float u0 = asf(uu.x << 16), u1 = asf(uu.x & 0xffff0000u), u2 = asf(uu.y << 16), u3 = asf(uu.y & 0xffff0000u);
        const v2u o = {pk(bfround(y0[0]) + bfround(u0 * dsk), bfround(y0[1]) + bfround(u1 * dsk)), pk(bfround(y0[2]) + bfround(u2 * dsk), bfround(y0[3]) + bfround(u3 * dsk))};
        bar();
        *(LAS v2u*)up = o;
    }
    bar();
#pragma unroll
    for (int it = 0; it < 4; ++it) {
        const int l = tid + 512 * it;
        unsigned e[8];
#pragma unroll
        for (int c = 0; c < 8; ++c) e[c] = *(LAS unsigned short*)(UT + c * UST + (UPAD + l) * 2);
        *(v4u*)((char*)P.out + (size_t)l * 2560 + d0 * 2) = v4u{e[0] | (e[1] << 16), e[2] | (e[3] << 16), e[4] | (e[5] << 16), e[6] | (e[7] << 16)};
    }
}
constexpr int NG = 64;
__device__ __forceinline__ void role_g(const Params& P, lds_t L, int g) {
    if (P.rolemask & 32) ssm_job(P, L, 8 * g);
    gstamp(P, 192 + g, 2);
    if (P.rolemask & 64) {
        const rsrc_t ws = mkrs(P.ws, WS_USED);
        for (int j = g; j < NSEG * 10; j += NG) { if (!final_job(P, ws, L, j / 10, j % 10)) return; }
    }
}

__device__ __forceinline__ int claim_role(const Params& P, lds_t L) {
    if (threadIdx.x == 0) {
        const rsrc_t ws = mkrs(P.ws, WS_USED);
        int role = blockIdx.x;
        if (!(P.dbg & 8)) {
            const unsigned xcc = (unsigned)__builtin_amdgcn_s_getreg(20 | (3 << 11)) & 7u;
            const unsigned t = __hip_atomic_fetch_add((GAS unsigned*)(P.ws + C_TICK + xcc * 128), 1u, __ATOMIC_RELAXED, __HIP_MEMORY_SCOPE_AGENT) + 1u;
            asm volatile("s_waitcnt vmcnt(0)" ::: "memory");
            __hip_atomic_fetch_add((GAS unsigned*)(P.ws + C_ARR), 1u, __ATOMIC_RELAXED, __HIP_MEMORY_SCOPE_AGENT);
            if (t < 32u) role = (int)(xcc * 32 + t);
            else {
                role = -1;
                unsigned spins = 0;
                for (;;) { const unsigned a = ld32s(ws, C_ARR) + 1u; if (a >= 256u) break; __builtin_amdgcn_s_sleep(4); if (++spins > SPIN_CAP) { set_abort(ws, 0x10); break; } }
                if (spins <= SPIN_CAP) {
                    unsigned k = __hip_atomic_fetch_add((GAS unsigned*)(P.ws + C_OVF), 1u, __ATOMIC_RELAXED, __HIP_MEMORY_SCOPE_AGENT) + 1u;
                    for (int j = 0; j < 8; ++j) { const unsigned n = ld32s(ws, C_TICK + j * 128) + 1u, fr = n < 32u ? 32u - n : 0u; if (role < 0) { if (k < fr) role = j * 32 + (int)(n + k); else k -= fr; } }
                }
            }
        } else role = (blockIdx.x & 7) * 32 + (blockIdx.x >> 3);
        *(LAS int*)L = role;
    }
    __syncthreads();
    const int role = __builtin_amdgcn_readfirstlane(*(LAS int*)L);
    __syncthreads();
    return role;
}
__device__ __forceinline__ int near_parity(const Params& P, bool leader) {
    const rsrc_t ws = mkrs(P.ws, WS_USED);
    if (P.np >= 0) return P.np;
    if (leader && threadIdx.x < 64) {
        unsigned t[2];
#pragma unroll
        for (int par = 0; par < 2; ++par) {
            unsigned best = 0xFFFFFFFFu;
#pragma unroll
            for (int r = 0; r < 4; ++r) {
                unsigned long long c0, c1; unsigned v;
                asm volatile("s_memtime %0\n\ts_waitcnt lgkmcnt(0)" : "=s"(c0) :: "memory");
                v = ld32s(ws, C_PROBE + par * BLK + r * 1024); asm volatile("s_waitcnt vmcnt(0)" : "+v"(v) :: "memory");
                asm volatile("s_memtime %0\n\ts_waitcnt lgkmcnt(0)" : "=s"(c1) :: "memory");
                const unsigned d = (unsigned)(c1 - c0); if (r > 0 && d < best) best = d;
            }
            t[par] = best;
        }
        const unsigned a0 = ((C_PROBE >> 13) & 1);
        const unsigned np = t[0] <= t[1] ? a0 : (a0 ^ 1);
        if (threadIdx.x == 0) st32s(ws, C_NP, np);
    }
    unsigned spins = 0;
    for (;;) { asm volatile("" ::: "memory"); const unsigned v = __builtin_amdgcn_readfirstlane(ld32s(ws, C_NP)); if (v != 0xFFFFFFFFu) return (int)(v & 1); __builtin_amdgcn_s_sleep(2); if (!spin_more(ws, spins, 0x20)) return 0; }
}

__global__ void __launch_bounds__(512, 2) k_main(Params P) {
    extern __shared__ __attribute__((aligned(16))) char smem[];
    lds_t L = (lds_t)smem;
    const unsigned t_entry = STAMPLITE ? (unsigned)__builtin_amdgcn_s_memrealtime() : 0u;
    const int role = claim_role(P, L);
    if (role < 0) return;
    if (STAMPLITE && threadIdx.x == 0) ((unsigned*)(P.ws + OFF_DBG + 10 * MB))[role * 8 + 0] = t_entry;
    gstamp(P, role, 1);
    const int cls = role >> 5, r = role & 31;
    if (cls < 4) {
        if (r < 8) { if (P.rolemask & 1) role_qk(P, L, 8 * cls + r, cls, near_parity(P, role == 0)); }
        else if (r < 16) { if (P.rolemask & 2) role_vo(P, L, 8 * cls + r - 8, cls, near_parity(P, false)); }
        else if (r < 26) { if (P.rolemask & 4) role_red(P, L, 10 * cls + r - 16, cls, near_parity(P, false)); }
        else if (r < 30) { if (P.rolemask & 32) ssm_job(P, L, 8 * (64 + 4 * cls + r - 26)); }
    } else {
        if (r < 8) { if (P.rolemask & 8) role_xqk(P, L, 8 * (cls - 4) + r); }
        else if (r < 16) { if (P.rolemask & 16) role_xv(P, L, 8 * (cls - 4) + r - 8); }
        else role_g(P, L, 16 * (cls - 4) + r - 16);
    }
    gstamp(P, role, 4);
}

}

extern "C" void kernel_launch(void* const* d_in, const int* in_sizes, int n_in, void* d_out, int out_size, void* d_ws, size_t ws_size, hipStream_t stream) {
    char* base = (char*)(((uintptr_t)d_ws + (2u << 20) - 1) & ~(uintptr_t)((2u << 20) - 1));
    Params P;
    P.x = d_in[9]; P.mem0 = d_in[10]; P.Wq = d_in[11]; P.Wk = d_in[12]; P.Wv = d_in[13]; P.Wo = d_in[14];
    P.u = d_in[0]; P.Are = d_in[1]; P.Aim = d_in[2]; P.Bre = d_in[3]; P.Bim = d_in[4]; P.Cre = d_in[5]; P.Cim = d_in[6]; P.logdt = d_in[7]; P.Dsk = d_in[8];
    P.ws = base; P.out = d_out; P.np = -1; P.dbg = 0; P.cq = TUNE_CQ; P.dzx = TUNE_DZX; P.pad0 = 0; P.dq = TUNE_DQ; P.dr = TUNE_DR; P.dz = TUNE_DZ; P.retry = TUNE_RETRY; P.rolemask = 127;
    (void)hipFuncSetAttribute((const void*)k_main, hipFuncAttributeMaxDynamicSharedMemorySize, LDS_BYTES);
    (void)hipMemsetAsync(base, 0xFF, MEMSET_BYTES, stream);
    hipLaunchKernelGGL(k_main, dim3(256), dim3(512), LDS_BYTES, stream, P);
}
```

```cpp
#include <hip/hip_runtime.h>
#include <hip/hip_bf16.h>

namespace {
constexpr int TUNE_DQ = 5, TUNE_DR = 5, TUNE_DZ = 11, TUNE_RETRY = 1, TUNE_CQ = 0, TUNE_DZX = 0;
#define DEVDBG 0
#define QCANK 10
#define RCAN 13
#define VCAN 1
#define RSTAMPS 0
#define SKEWST 0
#define TLBW 0
#define NTW 0
#define ZPOS 8
#define KXPRE 1
#define ONEACC 1
#define CINIT 0
#define RPRIO 1
#define DPERM 8
#define VONE 1
#define TLB0 0
#define SK0 30
#define PCNT 1
#define REVX 1
#define STAMPLITE 0
#define STAMPS 0

#define LAS __attribute__((address_space(3)))
#define GAS __attribute__((address_space(1)))
typedef unsigned v4u __attribute__((ext_vector_type(4)));
typedef unsigned v2u __attribute__((ext_vector_type(2)));
typedef float f32x4 __attribute__((ext_vector_type(4)));
typedef float f32x2 __attribute__((ext_vector_type(2)));
typedef short s16x4 __attribute__((ext_vector_type(4)));
typedef __bf16 bf16x2 __attribute__((ext_vector_type(2)));
typedef __bf16 bf16x8 __attribute__((ext_vector_type(8)));
typedef __bf16 bf16x16 __attribute__((ext_vector_type(16)));
typedef unsigned v8u __attribute__((ext_vector_type(8)));
typedef __amdgpu_buffer_rsrc_t rsrc_t;
typedef LAS char* lds_t;

constexpr int NSEG = 64, DM = 640, NH = 32, DH = 128, SEQ = 2048;
constexpr unsigned BLK = 8192;
constexpr unsigned J_ZM = 0, J_M = 80, J_PX = 104, J_PM = 120, J_END = 124;
constexpr unsigned OFF_CTL = 2 * J_END * BLK;
constexpr unsigned MEMSET_BYTES = OFF_CTL + 6 * BLK;
constexpr unsigned C_ABORT = OFF_CTL, C_NP = OFF_CTL + 128, C_TICK = OFF_CTL + 256  , C_ARR = OFF_CTL + 256 + 8 * 128, C_OVF = C_ARR + 128;
constexpr unsigned C_CNTX = OFF_CTL + 2048, C_CNTZ = C_CNTX + 64 * 128, C_CNTP = C_CNTZ + 64 * 128, C_CNTZM = C_CNTP + 64 * 128;
constexpr unsigned C_PROBE = OFF_CTL + 5 * BLK - 4096;
static_assert(C_CNTZM + 64 * 128 <= C_PROBE && C_PROBE + BLK + 4096 <= MEMSET_BYTES, "ctl");
constexpr unsigned MB = 1u << 20;
constexpr unsigned OFF_KX = 2 * MB, OFF_QX = 18 * MB, OFF_ZX = 34 * MB, OFF_ZMP = 114 * MB, OFF_PXX = 134 * MB, OFF_PXM = 138 * MB, OFF_DBG = 140 * MB, OFF_SSM = 150 * MB;
constexpr unsigned WS_USED = 200 * MB;
constexpr int AUX_SC1 = 16;
constexpr unsigned OOB = 0x80000000u;
constexpr unsigned SPIN_CAP = 1u << 15;
constexpr int LDS_BYTES = 152 * 1024;

struct Params {
    const void* x; const void* mem0; const void* Wq; const void* Wk; const void* Wv; const void* Wo;
    const void* u; const void* Are; const void* Aim; const void* Bre; const void* Bim; const void* Cre; const void* Cim; const void* logdt; const void* Dsk;
    char* ws;
    void* out;
    int rolemask; int np; int dbg; int dq; int dr; int dz; int retry; int cq; int dzx; int pad0;
};

__device__ __forceinline__ rsrc_t mkrs(const void* p, unsigned bytes) { return __builtin_amdgcn_make_buffer_rsrc((void*)p, 0, bytes, 0x00020000); }
__device__ __forceinline__ v4u ld128(rsrc_t rs, unsigned off, int aux) { return aux ? __builtin_amdgcn_raw_buffer_load_b128(rs, off, 0, AUX_SC1) : __builtin_amdgcn_raw_buffer_load_b128(rs, off, 0, 0); }
__device__ __forceinline__ v4u ld128s(rsrc_t rs, unsigned off) { return __builtin_amdgcn_raw_buffer_load_b128(rs, off, 0, AUX_SC1); }
__device__ __forceinline__ v2u ld64s(rsrc_t rs, unsigned off) { return __builtin_amdgcn_raw_buffer_load_b64(rs, off, 0, AUX_SC1); }
__device__ __forceinline__ unsigned ld32s(rsrc_t rs, unsigned off) { return __builtin_amdgcn_raw_buffer_load_b32(rs, off, 0, AUX_SC1); }
__device__ __forceinline__ v4u ld128so(rsrc_t rs, unsigned voff, unsigned soff) { return __builtin_amdgcn_raw_buffer_load_b128(rs, voff, soff, AUX_SC1); }
__device__ __forceinline__ v2u ld64so(rsrc_t rs, unsigned voff, unsigned soff) { return __builtin_amdgcn_raw_buffer_load_b64(rs, voff, soff, AUX_SC1); }
__device__ __forceinline__ void st128so(rsrc_t rs, unsigned voff, unsigned soff, v4u v) { __builtin_amdgcn_raw_buffer_store_b128(v, rs, voff, soff, AUX_SC1); }
__device__ __forceinline__ void st64so(rsrc_t rs, unsigned voff, unsigned soff, v2u v) { __builtin_amdgcn_raw_buffer_store_b64(v, rs, voff, soff, AUX_SC1); }
__device__ __forceinline__ unsigned umax3(unsigned a, unsigned b, unsigned c) { return max(max(a, b), c); }
__device__ __forceinline__ unsigned umax4(unsigned m, v4u v) { return umax3(umax3(m, v.x, v.y), v.z, v.w); }
__device__ __forceinline__ void st128s(rsrc_t rs, unsigned off, v4u v) { __builtin_amdgcn_raw_buffer_store_b128(v, rs, off, 0, AUX_SC1); }
__device__ __forceinline__ void st64s(rsrc_t rs, unsigned off, v2u v) { __builtin_amdgcn_raw_buffer_store_b64(v, rs, off, 0, AUX_SC1); }
__device__ __forceinline__ void st32s(rsrc_t rs, unsigned off, unsigned v) { __builtin_amdgcn_raw_buffer_store_b32(v, rs, off, 0, AUX_SC1); }
__device__ __forceinline__ v4u mksrd(const void* p, unsigned bytes) { const unsigned long long a = (unsigned long long)p; return v4u{(unsigned)a, (unsigned)(a >> 32) & 0xffffu, bytes, 0x00020000u}; }
__device__ __forceinline__ void dma16(v4u srd, lds_t dst, unsigned voff) {
    unsigned keep; const unsigned la = (unsigned)(unsigned long long)dst;
    asm volatile("s_mov_b32 %0, m0\n\ts_mov_b32 m0, %3\n\ts_nop 0\n\tbuffer_load_dwordx4 %1, %2, 0 offen lds\n\ts_mov_b32 m0, %0" : "=&s"(keep) : "v"(voff), "s"(srd), "s"(la) : "memory");
}
__device__ __forceinline__ void dma16w(v4u srd, lds_t dst, unsigned voff) {
    unsigned keep; const unsigned la = (unsigned)(unsigned long long)dst;
    if (NTW) asm volatile("s_mov_b32 %0, m0\n\ts_mov_b32 m0, %3\n\ts_nop 0\n\tbuffer_load_dwordx4 %1, %2, 0 offen nt lds\n\ts_mov_b32 m0, %0" : "=&s"(keep) : "v"(voff), "s"(srd), "s"(la) : "memory");
    else dma16(srd, dst, voff);
}
__device__ __forceinline__ void dma16s(v4u srd, lds_t dst, unsigned voff, unsigned soff) {
    unsigned keep; const unsigned la = (unsigned)(unsigned long long)dst;
    asm volatile("s_mov_b32 %0, m0\n\ts_mov_b32 m0, %3\n\ts_nop 0\n\tbuffer_load_dwordx4 %1, %2, %4 offen sc1 lds\n\ts_mov_b32 m0, %0" : "=&s"(keep) : "v"(voff), "s"(srd), "s"(la), "s"(soff) : "memory");
}
__device__ __forceinline__ void bar() { asm volatile("s_waitcnt lgkmcnt(0)\n\ts_barrier" ::: "memory"); }
__device__ __forceinline__ void drain() { asm volatile("s_waitcnt vmcnt(0)" ::: "memory"); }
__device__ __forceinline__ unsigned pk(float a, float b) { f32x2 v = {a, b}; return __builtin_bit_cast(unsigned, __builtin_convertvector(v, bf16x2)); }
__device__ __forceinline__ v4u mk_ones() { v4u o; asm volatile("v_mov_b32 %0, -1\n\tv_mov_b32 %1, -1\n\tv_mov_b32 %2, -1\n\tv_mov_b32 %3, -1" : "=v"(o.x), "=v"(o.y), "=v"(o.z), "=v"(o.w)); return o; }
__device__ __forceinline__ unsigned safe(unsigned x) { return x < 0xFFFFFFFEu ? x : 0xFFFFFFFEu; }
__device__ __forceinline__ bool valid4(v4u v) { return (v.x != 0xFFFFFFFFu) & (v.y != 0xFFFFFFFFu) & (v.z != 0xFFFFFFFFu) & (v.w != 0xFFFFFFFFu); }
__device__ __forceinline__ bool valid2(v2u v) { return (v.x != 0xFFFFFFFFu) & (v.y != 0xFFFFFFFFu); }
__device__ __forceinline__ v2u trd(lds_t p) { return __builtin_bit_cast(v2u, __builtin_amdgcn_ds_read_tr16_b64_v4i16((LAS s16x4*)p)); }
__device__ __forceinline__ f32x4 smf(v4u a, v8u b, f32x4 c, int idx) { return __builtin_amdgcn_smfmac_f32_16x16x64_bf16(__builtin_bit_cast(bf16x8, a), __builtin_bit_cast(bf16x16, b), c, idx, 0, 0); }
__device__ __forceinline__ f32x4 mf32(v4u a, v4u b, f32x4 c) { return __builtin_amdgcn_mfma_f32_16x16x32_bf16(__builtin_bit_cast(bf16x8, a), __builtin_bit_cast(bf16x8, b), c, 0, 0, 0); }
__device__ __forceinline__ f32x4 mf16(v2u a, v2u b, f32x4 c) { return __builtin_amdgcn_mfma_f32_16x16x16bf16_1k(__builtin_bit_cast(s16x4, a), __builtin_bit_cast(s16x4, b), c, 0, 0, 0); }
template <int CTRL> __device__ __forceinline__ float dppf(float v) { return __builtin_bit_cast(float, __builtin_amdgcn_update_dpp(0, __builtin_bit_cast(int, v), CTRL, 0xf, 0xf, true)); }
template <int CTRL> __device__ __forceinline__ unsigned dppu(unsigned v) { return (unsigned)__builtin_amdgcn_update_dpp(0, (int)v, CTRL, 0xf, 0xf, true); }
__device__ __forceinline__ float asf(unsigned u) { return __builtin_bit_cast(float, u); }
__device__ __forceinline__ unsigned asu(float f) { return __builtin_bit_cast(unsigned, f); }
__device__ __forceinline__ float swap32_sum(float x) { auto r = __builtin_amdgcn_permlane32_swap(asu(x), asu(x), false, false); const unsigned a = r[0], b = r[1]; return asf(a) + asf(b); }
__device__ __forceinline__ float swap32_max(float x) { auto r = __builtin_amdgcn_permlane32_swap(asu(x), asu(x), false, false); const unsigned a = r[0], b = r[1]; return fmaxf(asf(a), asf(b)); }
__device__ __forceinline__ float swap16_sum(float x) { auto r = __builtin_amdgcn_permlane16_swap(asu(x), asu(x), false, false); const unsigned a = r[0], b = r[1]; return asf(a) + asf(b); }
__device__ __forceinline__ float swap16_max(float x) { auto r = __builtin_amdgcn_permlane16_swap(asu(x), asu(x), false, false); const unsigned a = r[0], b = r[1]; return fmaxf(asf(a), asf(b)); }
__device__ __forceinline__ float fold2(float a, float b) { auto r = __builtin_amdgcn_permlane32_swap(asu(a), asu(b), false, false); const unsigned x = r[0], y = r[1]; return asf(x) + asf(y); }
__device__ __forceinline__ float vmax(float a, float b) { float r; asm("v_max_f32 %0, %1, %2" : "=v"(r) : "v"(a), "v"(b)); return r; }
__device__ __forceinline__ float vmax3(float a, float b, float c) { float r; asm("v_max3_f32 %0, %1, %2, %3" : "=v"(r) : "v"(a), "v"(b), "v"(c)); return r; }
__device__ __forceinline__ float row_max3(float x) {
    float y;
    asm("s_nop 1\n\tv_max_f32_dpp %0, %1, %1 row_ror:8 row_mask:0xf bank_mask:0xf" : "=&v"(y) : "v"(x));
    { auto r = __builtin_amdgcn_permlane16_swap(asu(y), asu(y), false, false); const unsigned a = r[0], b = r[1]; y = vmax(asf(a), asf(b)); }
    { auto r = __builtin_amdgcn_permlane32_swap(asu(y), asu(y), false, false); const unsigned a = r[0], b = r[1]; y = vmax(asf(a), asf(b)); }
    return y;
}
__device__ __forceinline__ float wave_max(float x) {
    float y;
    asm("s_nop 1\n\tv_max_f32_dpp %0, %1, %1 row_ror:8 row_mask:0xf bank_mask:0xf\n\ts_nop 1\n\tv_max_f32_dpp %0, %0, %0 row_ror:4 row_mask:0xf bank_mask:0xf\n\ts_nop 1\n\t"
        "v_max_f32_dpp %0, %0, %0 row_ror:2 row_mask:0xf bank_mask:0xf\n\ts_nop 1\n\tv_max_f32_dpp %0, %0, %0 row_ror:1 row_mask:0xf bank_mask:0xf\n\ts_nop 1" : "=&v"(y) : "v"(x));
    { auto r = __builtin_amdgcn_permlane16_swap(asu(y), asu(y), false, false); const unsigned a = r[0], b = r[1]; y = vmax(asf(a), asf(b)); }
    { auto r = __builtin_amdgcn_permlane32_swap(asu(y), asu(y), false, false); const unsigned a = r[0], b = r[1]; y = vmax(asf(a), asf(b)); }
    return y;
}
__device__ __forceinline__ float wave_sum(float x) { x += dppf<0x128>(x); x += dppf<0x124>(x); x += dppf<0x122>(x); x += dppf<0x121>(x); x = swap16_sum(x); return swap32_sum(x); }

struct Ctx {
    rsrc_t ws; lds_t L; int lane, w; int np;
};
__device__ __forceinline__ unsigned nblk(int j, int np) { return (unsigned)(2 * j + np) * BLK; }
__device__ __forceinline__ unsigned zm_ring(int slot, int c, int np) { return nblk(J_ZM + slot * 40 + c, np); }
__device__ __forceinline__ unsigned m_base(int slot, int copy, int np) { return nblk(J_M + 2 * (slot * 4 + copy), np); }
__device__ __forceinline__ unsigned px_base(int slot, int copy, int np) { return nblk(J_PX + 2 * (slot * 4 + copy), np); }
__device__ __forceinline__ unsigned pm_base(int slot, int copy, int np) { const int i = slot * 4 + copy; return nblk(J_PM + (i >> 1), np) + (i & 1) * 4096; }
__device__ __forceinline__ unsigned can_off(int kind, int cls, int np) { return nblk(J_M + 1, np) + 4096 + (kind * 4 + cls) * 128; }
__host__ __device__ constexpr unsigned fragoff(int f) { return (unsigned)((f >> 3) * 16384 + (f & 7) * 1024); }

__device__ __forceinline__ bool aborted(rsrc_t ws) { return __builtin_amdgcn_readfirstlane(ld32s(ws, C_ABORT)) != 0xFFFFFFFFu; }
__device__ __forceinline__ void set_abort(rsrc_t ws, unsigned code) { st32s(ws, C_ABORT, code); }
__device__ __forceinline__ bool spin_more(rsrc_t ws, unsigned& spins, unsigned code) {
    ++spins;
    if (spins > SPIN_CAP) { set_abort(ws, code); return false; }
    if ((spins & 63u) == 0u && aborted(ws)) return false;
    return true;
}
__device__ __forceinline__ bool wait_cnt(rsrc_t ws, unsigned off, unsigned n, unsigned code) {
    unsigned spins = 0;
    for (;;) {
        asm volatile("" ::: "memory");
        unsigned v = __builtin_amdgcn_readfirstlane(ld32s(ws, off));
        if (v + 1u >= n) return true;
        __builtin_amdgcn_s_sleep(2);
        if (!spin_more(ws, spins, code)) return false;
    }
}
constexpr int STAMP_OFF = 148 * 1024;
struct TS {
    unsigned acc[12]; unsigned last; unsigned r0, c0;
    __device__ __forceinline__ static unsigned now() { unsigned long long t; asm volatile("s_memtime %0\n\ts_waitcnt lgkmcnt(0)" : "=s"(t) :: "memory"); return (unsigned)t; }
    __device__ __forceinline__ void init() { if (STAMPLITE) r0 = (unsigned)__builtin_amdgcn_s_memrealtime(); if (STAMPS) { for (int i = 0; i < 12; ++i) acc[i] = 0; r0 = (unsigned)__builtin_amdgcn_s_memrealtime(); c0 = now(); last = c0; } }
    __device__ __forceinline__ void mark(int i, bool on = true) { if (STAMPS) { __builtin_amdgcn_sched_barrier(0); const unsigned t = now(); if (on) acc[i] += t - last; last = t; __builtin_amdgcn_sched_barrier(0); } }
    __device__ __forceinline__ void initR() { if (RSTAMPS) { for (int i = 0; i < 12; ++i) acc[i] = 0; last = now(); } }
    __device__ __forceinline__ void markR(int i, bool on) { if (RSTAMPS) { __builtin_amdgcn_sched_barrier(0); const unsigned t = now(); if (on) acc[i] += t - last; last = t; __builtin_amdgcn_sched_barrier(0); } }
    __device__ __forceinline__ void dumpR(const Params& P, int who) { if (RSTAMPS && (threadIdx.x & 63) == 0) { unsigned* o = (unsigned*)(P.ws + OFF_DBG + 11 * MB) + who * 16; for (int i = 0; i < 12; ++i) o[i] = acc[i]; } }
    __device__ __forceinline__ void dump(const Params& P, int who, unsigned extra) {
        if (STAMPLITE) { const unsigned r1 = (unsigned)__builtin_amdgcn_s_memrealtime(); const rsrc_t w_ = mkrs(P.ws, WS_USED); const unsigned vo = (threadIdx.x & 63) ? OOB : (threadIdx.x >> 6) * 64, so = OFF_DBG + 8 * MB + who * 512;
            __builtin_amdgcn_raw_buffer_store_b32(r1 - r0, w_, vo, so + 48, 0); __builtin_amdgcn_raw_buffer_store_b32(24 * (r1 - r0), w_, vo, so + 52, 0); __builtin_amdgcn_raw_buffer_store_b32(extra, w_, vo, so + 56, 0); }
        if (STAMPS) { const unsigned r1 = (unsigned)__builtin_amdgcn_s_memrealtime(), c1 = now();
            if ((threadIdx.x & 63) == 0) { unsigned* o = (unsigned*)(P.ws + OFF_DBG + 8 * MB) + (who * 8 + (threadIdx.x >> 6)) * 16; for (int i = 0; i < 12; ++i) o[i] = acc[i]; o[12] = r1 - r0; o[13] = c1 - c0; o[14] = extra; } }
    }
};
__device__ __forceinline__ void cnt_add(char* wsbase, unsigned off) { __hip_atomic_fetch_add((GAS unsigned*)(wsbase + off), 1u, __ATOMIC_RELAXED, __HIP_MEMORY_SCOPE_AGENT); }

__device__ __forceinline__ void gstamp(const Params& P, int role, int k) { if (STAMPLITE) { const unsigned t_ = (unsigned)__builtin_amdgcn_s_memrealtime(); __builtin_amdgcn_raw_buffer_store_b32(t_, mkrs(P.ws, WS_USED), threadIdx.x ? OOB : 0u, OFF_DBG + 10 * MB + (role * 8 + k) * 4, 0); } }
template <int SIG> __device__ __forceinline__ int sigma(int row) {
    if (SIG == 0) return (row & 1) | (((row >> 2) & 1) << 1) | (((row >> 3) & 1) << 2);
    if (SIG == 1) return (row & 3) | (((row >> 3) & 1) << 2);
    return row & 7;
}
template <int SIG> __device__ __forceinline__ void stage_rows256(v4u rw, lds_t dst, int row0, int h, int w, int lane) {
#pragma unroll
    for (int i = 0; i < 2; ++i) {
        const int ins = 2 * w + i, r = 4 * ins + (lane >> 4), s = lane & 15;
        const int col = (((s >> 1) ^ sigma<SIG>(r)) << 1) | (s & 1);
        dma16w(rw, dst + ins * 1024, (unsigned)(row0 + r) * 8192u + (unsigned)h * 256u + (unsigned)col * 16u);
    }
}
template <int SIG> __device__ __forceinline__ void stage_rows1280(v4u rw, lds_t dst, int e0, int h, int w, int lane) {
#pragma unroll
    for (int i = 0; i < 10; ++i) {
        const int ins = 10 * w + i, S = ins * 64 + lane, r = S / 80, s = S % 80, s16 = s & 15;
        const int col = (s & ~15) | ((((s16 >> 1) ^ sigma<SIG>(r)) << 1) | (s16 & 1));
        dma16w(rw, dst + ins * 1024, (unsigned)(h * 128 + e0 + r) * 1280u + (unsigned)col * 16u);
    }
}
template <int SIG> __device__ __forceinline__ void stage_rows1280h(v4u rw, lds_t dst, int e0, int h, int w, int lane) {
#pragma unroll
    for (int i = 0; i < 5; ++i) {
        const int ins = 5 * w + i, S = ins * 64 + lane, r = S / 80, s = S % 80, s16 = s & 15;
        const int col = (s & ~15) | ((((s16 >> 1) ^ sigma<SIG>(r)) << 1) | (s16 & 1));
        dma16w(rw, dst + ins * 1024, (unsigned)(h * 128 + e0 + r) * 1280u + (unsigned)col * 16u);
    }
}
__device__ __forceinline__ v4u rd_sparseBh(lds_t base, int stride, int pair, int lane) {
    const int g = lane >> 4, q = (lane >> 2) & 3, p = lane & 3;
    v4u o;
#pragma unroll
    for (int rho = 0; rho < 2; ++rho) {
        const int row = 16 * (g >> 1) + 4 * (g & 1) + 2 * rho + (q & 1) + 8 * (q >> 1);
        const v2u t = trd(base + row * stride + (pair >> 3) * 256 + (((pair & 7) ^ sigma<0>(row)) * 32) + 8 * p);
        o[2 * rho] = t.x; o[2 * rho + 1] = t.y;
    }
    return o;
}
__device__ __forceinline__ void wait_fills(int n) {
    if (n == 0) asm volatile("s_waitcnt vmcnt(0)" ::: "memory");
    else if (n == 4) asm volatile("s_waitcnt vmcnt(4)" ::: "memory");
    else if (n == 5) asm volatile("s_waitcnt vmcnt(5)" ::: "memory");
    else if (n == 8) asm volatile("s_waitcnt vmcnt(8)" ::: "memory");
    else if (n == 9) asm volatile("s_waitcnt vmcnt(9)" ::: "memory");
    else asm volatile("s_waitcnt vmcnt(10)" ::: "memory");
}
__device__ __forceinline__ v8u rd_sparseB(lds_t base, int stride, int pair, int lane) {
    const int g = lane >> 4, q = (lane >> 2) & 3, p = lane & 3;
    v8u o;
#pragma unroll
    for (int b = 0; b < 2; ++b)
#pragma unroll
        for (int rho = 0; rho < 2; ++rho) {
            const int row = 32 * b + 16 * (g >> 1) + 4 * (g & 1) + 2 * rho + (q & 1) + 8 * (q >> 1);
            const v2u t = trd(base + row * stride + (pair >> 3) * 256 + (((pair & 7) ^ sigma<0>(row)) * 32) + 8 * p);
            o[4 * b + 2 * rho] = t.x; o[4 * b + 2 * rho + 1] = t.y;
        }
    return o;
}
__device__ __forceinline__ v4u rd_denseNat(lds_t base, int stride, int row0, int pair, int lane) {
    const int g = lane >> 4, q = (lane >> 2) & 3, p = lane & 3;
    v4u o;
#pragma unroll
    for (int rho = 0; rho < 2; ++rho) {
        const int row = row0 + 8 * g + 4 * rho + q;
        const v2u t = trd(base + row * stride + (pair >> 3) * 256 + (((pair & 7) ^ sigma<1>(row)) * 32) + 8 * p);
        o[2 * rho] = t.x; o[2 * rho + 1] = t.y;
    }
    return o;
}
__device__ __forceinline__ v4u rd_densePerm(lds_t base, int stride, int row0, int pair, int lane) {
    const int g = lane >> 4, q = (lane >> 2) & 3, p = lane & 3;
    v4u o;
#pragma unroll
    for (int rho = 0; rho < 2; ++rho) {
        const int row = row0 + 16 * rho + 4 * g + q;
        const v2u t = trd(base + row * stride + (pair >> 3) * 256 + (((pair & 7) ^ sigma<2>(row)) * 32) + 8 * p);
        o[2 * rho] = t.x; o[2 * rho + 1] = t.y;
    }
    return o;
}

__device__ __forceinline__ unsigned rtime() { return (unsigned)__builtin_amdgcn_s_memrealtime(); }
__device__ __forceinline__ void skst(rsrc_t ws, int lane, int idx) { const unsigned t_ = rtime(); __builtin_amdgcn_raw_buffer_store_b32(t_, ws, lane ? OOB : 0u, OFF_DBG + 9 * MB + idx * 4, 0); }
__device__ __forceinline__ bool canary_wait(rsrc_t ws, unsigned off, int want, int dly, unsigned code) {
    unsigned v0 = ld32s(ws, off); __builtin_amdgcn_s_sleep(1);
    unsigned v1 = ld32s(ws, off); __builtin_amdgcn_s_sleep(1);
    unsigned v2 = ld32s(ws, off); __builtin_amdgcn_s_sleep(1);
    unsigned v3 = ld32s(ws, off);
    unsigned spins = 0;
    for (;;) {
        if ((int)__builtin_amdgcn_readfirstlane(v0) >= want) break;
        asm volatile("" ::: "memory"); v0 = ld32s(ws, off);
        if ((int)__builtin_amdgcn_readfirstlane(v1) >= want) break;
        asm volatile("" ::: "memory"); v1 = ld32s(ws, off);
        if ((int)__builtin_amdgcn_readfirstlane(v2) >= want) break;
        asm volatile("" ::: "memory"); v2 = ld32s(ws, off);
        if ((int)__builtin_amdgcn_readfirstlane(v3) >= want) break;
        asm volatile("" ::: "memory"); v3 = ld32s(ws, off);
        if (!spin_more(ws, spins, code)) return false;
    }
    for (int z = 0; z < dly; ++z) asm volatile("s_nop 3");
    return true;
}

__device__ __forceinline__ void stage_mem0(const Params& P, lds_t Mb) {
    const rsrc_t rm = mkrs(P.mem0, 8 * DM * 2);
#pragma unroll
    for (int it = 0; it < 2; ++it) {
        const int d = threadIdx.x + 512 * it;
        if (d < DM) {
            unsigned v[8];
#pragma unroll
            for (int i = 0; i < 8; ++i) v[i] = __builtin_amdgcn_raw_buffer_load_b16(rm, (i * DM + d) * 2, 0, 0);
            *(LAS v4u*)(Mb + (d >> 4) * 256 + ((d & 15) ^ (4 * ((d >> 4) & 1))) * 16) = v4u{v[0] | (v[1] << 16), v[2] | (v[3] << 16), v[4] | (v[5] << 16), v[6] | (v[7] << 16)};
        }
    }
}
__device__ __forceinline__ bool gather_m(const Params& P, rsrc_t ws, lds_t Mb, int s, int cls, int w, int lane, int np, unsigned& nfail) {
    if (s == 0) return true;
    const bool two = w >= 6;
    const int f2 = w + 2;
    const unsigned sb = m_base(s % 3, cls, np);
    const unsigned oa = w * 1024 + lane * 16, ob = two ? 16384 + (w - 6) * 1024 + lane * 16 : OOB;
    if (w == 6) { if (!canary_wait(ws, can_off(0, cls, np), s, P.dq, 0x180 + s)) return false; }
    asm volatile("s_barrier" ::: "memory");
    v4u a = ld128so(ws, oa, sb), b = ld128so(ws, ob, sb);
    *(LAS v4u*)(Mb + w * 1024 + lane * 16) = a;
    if (two) *(LAS v4u*)(Mb + f2 * 1024 + lane * 16) = b;
    if (__builtin_expect(!__all(umax4(umax4(0u, a), b) != 0xFFFFFFFFu), 0)) {
        unsigned spins = 0;
        ++nfail;
        for (;;) {
            asm volatile("" ::: "memory");
            for (int z = 0; z < P.retry; ++z) __builtin_amdgcn_s_sleep(1);
            if (!valid4(a)) a = ld128so(ws, oa, sb);
            if (!valid4(b)) b = ld128so(ws, ob, sb);
            if (__all(valid4(a) && valid4(b))) break;
            if (!spin_more(ws, spins, 0x100 + s)) return false;
        }
        *(LAS v4u*)(Mb + w * 1024 + lane * 16) = a;
        if (two) *(LAS v4u*)(Mb + f2 * 1024 + lane * 16) = b;
    }
    return true;
}
__device__ __forceinline__ unsigned afrag_off(int lane) { const int g = lane >> 4, q = (lane >> 2) & 3, p = lane & 3; return g * 256 + ((8 * (p >> 1) + q) ^ (4 * (g & 1))) * 16 + (p & 1) * 8; }
__device__ __forceinline__ v4u rd_afrag(lds_t base, unsigned a0, int k) { const v2u x = trd(base + k * 1024 + a0), y = trd(base + k * 1024 + (a0 ^ 64)); return v4u{x.x, x.y, y.x, y.y}; }

__device__ __forceinline__ void role_qk(const Params& P, lds_t L, int h, int cls, int np) {
    const int tid = threadIdx.x, lane = tid & 63, w = __builtin_amdgcn_readfirstlane(tid >> 6);
    const rsrc_t ws = mkrs(P.ws, WS_USED);
    const v4u rq = mksrd(P.Wq, DM * 4096 * 2), rk = mksrd(P.Wk, DM * 4096 * 2), wsd = mksrd(P.ws, WS_USED);
    v8u Wq[10], Wk[10];
    {
        auto stage = [&](int st) {
            lds_t b = L + (st & 1) * 65536;
            stage_rows256<0>(rq, b, 128 * st, h, w, lane); stage_rows256<0>(rq, b + 16384, 128 * st + 64, h, w, lane);
            stage_rows256<0>(rk, b + 32768, 128 * st, h, w, lane); stage_rows256<0>(rk, b + 49152, 128 * st + 64, h, w, lane);
        };
        stage(0); stage(1);
#pragma unroll
        for (int st = 0; st < 5; ++st) {
            if (st < 4) asm volatile("s_waitcnt vmcnt(8)" ::: "memory"); else drain();
            bar();
            lds_t b = L + (st & 1) * 65536;
            Wq[2 * st] = rd_sparseB(b, 256, w, lane); Wq[2 * st + 1] = rd_sparseB(b + 16384, 256, w, lane);
            Wk[2 * st] = rd_sparseB(b + 32768, 256, w, lane); Wk[2 * st + 1] = rd_sparseB(b + 49152, 256, w, lane);
            bar();
            if (st + 2 < 5) stage(st + 2);
        }
    }
    lds_t Mb = L;
    lds_t QT = L + 10240, KT = QT + 2048;
    lds_t XB = L + 28672;
    const unsigned a0 = afrag_off(lane);
    const int sidx = ((lane >> 3) & 1) ? 0xEEEE : 0x4444;
    const bool rowl = (lane & 15) < 8;
    const unsigned pxo = (w < 4 && rowl) ? px_base(0, w & 3, np) + ((h >> 1) >> 3) * 16384 + ((h >> 1) & 7) * 1024 + (h & 1) * 512 + (lane >> 4) * 128 + (lane & 7) * 16 : OOB;
    const unsigned pmo = (w < 4 && lane < 8) ? pm_base(0, w & 3, np) + (h >> 3) * 1024 + (h & 7) * 128 + lane * 16 : OOB;
    constexpr unsigned PXS = 8 * 2 * BLK, PMS = 2 * 2 * BLK;
    const int tro = (4 * (lane >> 4) + ((lane >> 2) & 3)) * 16 + (lane & 1) * 8;
    const unsigned xo = (unsigned)h * 8192u + lane * 16;
    auto dma_x = [&](int s) {
        lds_t xb = XB + (s & 1) * 16384;
#pragma unroll
        for (int k = 0; k < 4; ++k) { const int f = 4 * (w & 1) + k; dma16s(wsd, xb + (w & 2 ? 8192 : 0) + f * 1024, xo + f * 1024, ((w & 2) ? OFF_QX : OFF_KX) + (unsigned)s * (32u * 8192u)); }
    };
    stage_mem0(P, Mb);
    if (!wait_cnt(ws, C_CNTX, 32, 0x200)) return;
    if (w >= 4) dma_x(0);
    unsigned cx = 0;
    if (w >= 4) cx = ld32s(ws, C_CNTX + 128);
    constexpr float SC = 0.08838834764831845f * 1.4426950408889634f;
    unsigned nfail = 0;
    gstamp(P, h, 2);
    TS ts; ts.init();
    for (int s = 0; s < NSEG; ++s) {
        const bool on = s >= 8 && s < 60;
        if (!gather_m(P, ws, Mb, s, cls, w, lane, np, nfail)) return;
        ts.mark(0, on);
        drain();
        bar();
        if (s == 1) gstamp(P, h, 3);
        if (s == 63) gstamp(P, h, 5);
        if (SKEWST && w == 0 && s >= SK0 && s < SK0 + 8) skst(ws, lane, (h * 8 + (s - SK0)) * 4 + 0);
        ts.mark(1, on);
        const bool last = s == NSEG - 1;
        lds_t xb = XB + (s & 1) * 16384;
        v4u af[10];
#pragma unroll
        for (int k = 0; k < 2; ++k) { af[k] = rd_afrag(Mb, a0, k); __builtin_amdgcn_sched_barrier(0); }
        f32x4 aq = {0.f, 0.f, 0.f, 0.f}, ak = {0.f, 0.f, 0.f, 0.f};
#pragma unroll
        for (int k = 0; k < 10; ++k) {
            if (k + 2 < 10) af[k + 2] = rd_afrag(Mb, a0, k + 2);
            __builtin_amdgcn_sched_barrier(0);
            aq = smf(af[k], Wq[k], aq, sidx); ak = smf(af[k], Wk[k], ak, sidx);
            if (k == 1) {
                const bool dop = s >= 1 && !last; const v4u ones = mk_ones();
                st128so(ws, dop ? pxo : OOB, (s & 1) ? 0u : PXS, ones); st128so(ws, dop ? pmo : OOB, (s & 1) ? 0u : PMS, ones);
                if (PCNT == 0 && s >= 1 && tid == 448) cnt_add(P.ws, C_CNTP + (s - 1) * 128);
            }
            __builtin_amdgcn_sched_barrier(0);
        }
        const bool crit = w < 4 && !last;
        v4u kx[4][2];
        if (crit) {
#pragma unroll
            for (int k = 0; k < KXPRE; ++k) { kx[k][0] = *(LAS v4u*)(xb + (k * 2 + 0) * 1024 + lane * 16); kx[k][1] = *(LAS v4u*)(xb + (k * 2 + 1) * 1024 + lane * 16); }
        }
        const float v0 = fold2(aq[0], ak[0]), v1 = fold2(aq[1], ak[1]), v2 = fold2(aq[2], ak[2]), v3 = fold2(aq[3], ak[3]);
        *(LAS v2u*)(QT + (lane >> 5) * 2048 + (16 * w + (lane & 15)) * 16 + ((lane >> 4) & 1) * 8) = v2u{pk(v0, v1), pk(v2, v3)};
        ts.mark(2, on);
        bar();
        if (SKEWST && w == 0 && s >= SK0 && s < SK0 + 8) skst(ws, lane, (h * 8 + (s - SK0)) * 4 + 2);
        ts.mark(3, on);
        if ((DEVDBG && (P.dbg & 1)) && s == 0) { ((v2u*)(P.ws + OFF_DBG + 6 * MB + h * 4096))[tid] = *(LAS v2u*)(QT + tid * 8); }
        if (crit) {
            __builtin_amdgcn_s_setprio(2);
            v4u qf[4], km[4];
            auto rdqk = [&](int k) { const v2u a = trd(QT + k * 512 + tro), b = trd(QT + k * 512 + tro + 256), c = trd(KT + k * 512 + tro), d = trd(KT + k * 512 + tro + 256);
                                     qf[k] = v4u{a.x, a.y, b.x, b.y}; km[k] = v4u{c.x, c.y, d.x, d.y}; };
            auto rdkx = [&](int k) { if (k >= KXPRE) { kx[k][0] = *(LAS v4u*)(xb + (k * 2 + 0) * 1024 + lane * 16); kx[k][1] = *(LAS v4u*)(xb + (k * 2 + 1) * 1024 + lane * 16); } };
            rdqk(0); rdkx(0); __builtin_amdgcn_sched_barrier(0);
            rdqk(1); rdkx(1);
            __builtin_amdgcn_sched_barrier(0);
            const float ci = (!CINIT || (rowl && lane < 32)) ? 0.f : -INFINITY;
            f32x4 t0 = {0.f, 0.f, 0.f, 0.f}, t1 = t0, tm = {ci, ci, ci, ci};
#pragma unroll
            for (int k = 0; k < 4; ++k) {
                t0 = mf32(kx[k][0], qf[k], t0); t1 = mf32(kx[k][1], qf[k], t1); tm = mf32(km[k], qf[k], tm);
                __builtin_amdgcn_sched_barrier(0);
                if (k < 2) { rdqk(k + 2); rdkx(k + 2); }
                __builtin_amdgcn_sched_barrier(0);
            }
            float x[4], m[4];
#pragma unroll
            for (int r = 0; r < 4; ++r) {
                x[r] = asf((unsigned)__builtin_amdgcn_update_dpp((int)asu(t0[r]), (int)asu(t1[r]), 0x118, 0xf, 0xc, false));
                m[r] = (CINIT || (rowl && lane < 32)) ? tm[r] : -INFINITY;
            }
            float mx = vmax(vmax3(x[0], x[1], x[2]), vmax3(vmax3(x[3], m[0], m[1]), m[2], m[3]));
            mx = row_max3(mx);
            const float mc = mx * SC;
            float sum = 0.f;
#pragma unroll
            for (int r = 0; r < 4; ++r) { x[r] = __builtin_amdgcn_exp2f(__builtin_fmaf(x[r], SC, -mc)); m[r] = __builtin_amdgcn_exp2f(__builtin_fmaf(m[r], SC, -mc)); }
            sum = ((x[0] + x[1]) + (x[2] + x[3])) + ((m[0] + m[1]) + (m[2] + m[3]));
            sum += dppf<0x128>(sum); sum = swap32_sum(swap16_sum(sum));
            const float inv = __builtin_amdgcn_rcpf(sum);
            const unsigned xa = safe(pk(x[0] * inv, x[1] * inv)), xb2 = safe(pk(x[2] * inv, x[3] * inv)), ma = safe(pk(m[0] * inv, m[1] * inv)), mb = safe(pk(m[2] * inv, m[3] * inv));
            const unsigned ya = dppu<0x108>(xa), yb = dppu<0x108>(xb2);
            const auto ra = __builtin_amdgcn_permlane16_swap(ma, ma, false, false), rb = __builtin_amdgcn_permlane16_swap(mb, mb, false, false);
            drain();
            st128so(ws, pxo, (s & 1) ? PXS : 0u, v4u{xa, xb2, ya, yb});
            st128so(ws, pmo, (s & 1) ? PMS : 0u, v4u{ra[0], rb[0], ra[1], rb[1]});
            __builtin_amdgcn_s_setprio(0);
            if (SKEWST && w == 0 && s >= SK0 && s < SK0 + 8) skst(ws, lane, (h * 8 + (s - SK0)) * 4 + 1);
            if ((DEVDBG && (P.dbg & 1)) && w == 0) {
                float* o = (float*)(P.ws + OFF_DBG + 2 * MB) + ((s * 32 + h) * 8 + (lane & 7)) * 48;
#pragma unroll
                for (int r = 0; r < 4; ++r) { o[16 + 16 * ((lane >> 3) & 1) + 4 * (lane >> 4) + r] = x[r] * inv; if (rowl && lane < 32) o[4 * (lane >> 4) + r] = m[r] * inv; }
            }
        }
        if (w >= 4 && !last) {
            const unsigned cxs = __builtin_amdgcn_readfirstlane(cx);
            if (h == 0 && w == 7) { for (int z = 0; z < P.cq; ++z) asm volatile("s_nop 3"); int l2 = lane; asm volatile("" : "+v"(l2)); st32s(ws, l2 < 4 ? can_off(1, l2, np) : OOB, (unsigned)(s + 1)); }
            if (cxs + 1u < 32u) { if (!wait_cnt(ws, C_CNTX + (s + 1) * 128, 32, 0x200 + s)) return; }
            dma_x(s + 1);
            cx = ld32s(ws, s + 2 < NSEG ? C_CNTX + (s + 2) * 128 : OOB);
        }
        ts.mark(4, on);
        if (w == 4 || w == 5) {
            const int tq = w - 4;
            f32x4 tm = {0.f, 0.f, 0.f, 0.f}, t0 = tm, t1 = tm;
#pragma unroll
            for (int k = 0; k < 4; ++k) {
                const v4u qf = *(LAS v4u*)(xb + 8192 + (k * 2 + tq) * 1024 + lane * 16);
                const v4u k0 = *(LAS v4u*)(xb + (k * 2 + 0) * 1024 + lane * 16), k1 = *(LAS v4u*)(xb + (k * 2 + 1) * 1024 + lane * 16);
                const v2u ma = trd(KT + k * 512 + tro), mb = trd(KT + k * 512 + tro + 256);
                tm = mf32(v4u{ma.x, ma.y, mb.x, mb.y}, qf, tm); t0 = mf32(k0, qf, t0); t1 = mf32(k1, qf, t1);
            }
            const int ii = 16 * tq + (lane & 15), g = lane >> 4;
            float sv[12];
#pragma unroll
            for (int r = 0; r < 4; ++r) {
                sv[r] = g < 2 ? tm[r] : -INFINITY;
                sv[4 + r] = (4 * g + r <= ii) ? t0[r] : -INFINITY;
                sv[8 + r] = (16 + 4 * g + r <= ii) ? t1[r] : -INFINITY;
            }
            float mx = sv[0];
#pragma unroll
            for (int r = 1; r < 12; ++r) mx = fmaxf(mx, sv[r]);
            mx = swap32_max(swap16_max(mx));
            float sum = 0.f;
#pragma unroll
            for (int r = 0; r < 12; ++r) { sv[r] = __builtin_amdgcn_exp2f((sv[r] - mx) * SC); sum += sv[r]; }
            sum = swap32_sum(swap16_sum(sum));
            const float inv = __builtin_amdgcn_rcpf(sum);
            const auto r01 = __builtin_amdgcn_permlane16_swap(pk(sv[4] * inv, sv[5] * inv), pk(sv[8] * inv, sv[9] * inv), false, false);
            const auto r23 = __builtin_amdgcn_permlane16_swap(pk(sv[6] * inv, sv[7] * inv), pk(sv[10] * inv, sv[11] * inv), false, false);
            st128s(ws, OFF_PXX + (unsigned)((s * 32 + h) * 2 + tq) * 1024u + lane * 16, v4u{r01[0], r01[1], r23[0], r23[1]});
            st64s(ws, g < 2 ? OFF_PXM + (unsigned)(s * 32 + h) * 512u + ii * 16 + g * 8 : OOB, v2u{pk(sv[0] * inv, sv[1] * inv), pk(sv[2] * inv, sv[3] * inv)});
        }
        if (PCNT == 1 && s >= 1 && tid == 448) cnt_add(P.ws, C_CNTP + (s - 1) * 128);
        ts.mark(5, on);
    }
    drain(); bar();
    if (tid == 0) cnt_add(P.ws, C_CNTP + (NSEG - 1) * 128);
    ts.dump(P, h, nfail);
}

__device__ __forceinline__ void role_vo(const Params& P, lds_t L, int h, int cls, int np) {
    const int tid = threadIdx.x, lane = tid & 63, w = __builtin_amdgcn_readfirstlane(tid >> 6);
    const rsrc_t ws = mkrs(P.ws, WS_USED);
    const v4u rv = mksrd(P.Wv, DM * 4096 * 2), ro = mksrd(P.Wo, 4096 * DM * 2);
    v8u Wv[10], Wo[10];
    {
        auto stage = [&](int i) { lds_t b = L + (i % 3) * 40960;
            if (i < 5) { stage_rows256<0>(rv, b, 128 * i, h, w, lane); stage_rows256<0>(rv, b + 16384, 128 * i + 64, h, w, lane); } else stage_rows1280h<0>(ro, b, 32 * (i - 5), h, w, lane); };
        stage(0); stage(1); stage(2);
#pragma unroll
        for (int i = 0; i < 9; ++i) {
            wait_fills((i + 1 < 9 ? (i + 1 < 5 ? 4 : 5) : 0) + (i + 2 < 9 ? (i + 2 < 5 ? 4 : 5) : 0));
            bar();
            lds_t b = L + (i % 3) * 40960;
            if (i < 5) { Wv[2 * i] = rd_sparseB(b, 256, w, lane); Wv[2 * i + 1] = rd_sparseB(b + 16384, 256, w, lane); }
            else {
                const int kap = (i - 5) >> 1, blk = (i - 5) & 1;
#pragma unroll
                for (int t = 0; t < 5; ++t) { const v4u x = rd_sparseBh(b, 1280, 5 * w + t, lane);
                    Wo[2 * t + kap][4 * blk] = x.x; Wo[2 * t + kap][4 * blk + 1] = x.y; Wo[2 * t + kap][4 * blk + 2] = x.z; Wo[2 * t + kap][4 * blk + 3] = x.w; }
            }
            bar();
            if (i + 3 < 9) stage(i + 3);
        }
    }
    lds_t Mb = L, VT = L + 10240;
    const unsigned a0 = afrag_off(lane);
    const int sidx = ((lane >> 3) & 1) ? 0xEEEE : 0x4444;
    const int tile4 = ((lane >> 4) & 1) * 2 + (lane >> 5);
    const unsigned zo4 = (unsigned)h * 256 + (lane & 15) * 16, zo1 = lane < 32 ? (unsigned)h * 256 + (lane & 15) * 16 + (lane >> 4) * 8 : OOB;
    const unsigned r4 = (5 * w + tile4) * 16384 + zo4, r1 = lane < 32 ? (5 * w + 4) * 16384 + zo1 : OOB;
    const unsigned q4 = (5 * w + tile4) * 8192 + zo4, q1 = lane < 32 ? (5 * w + 4) * 8192 + zo1 : OOB;
    const unsigned zb0 = zm_ring(0, 0, np), zb1 = zm_ring(1, 0, np);
    lds_t vtw = VT + (16 * w + ((lane & 15) ^ (4 * (w & 1)))) * 16 + ((lane >> 4) & 1) * 8;
    const v4u ones = {0xFFFFFFFFu, 0xFFFFFFFFu, 0xFFFFFFFFu, 0xFFFFFFFFu};
    stage_mem0(P, Mb);
    unsigned nfail = 0;
    const unsigned cano = (h == 0 && w == 0 && lane < 4) ? can_off(2, lane, np) : OOB;
    TS ts; ts.init();
    for (int s = 0; s < NSEG; ++s) {
        const bool on = s >= 8 && s < 60;
        if (!gather_m(P, ws, Mb, s, cls, w, lane, np, nfail)) return;
        ts.mark(0, on);
        drain();
        bar();
        ts.mark(1, on);
        const bool last = s == NSEG - 1;
        v4u af[10];
#pragma unroll
        for (int k = 0; k < 3; ++k) { af[k] = rd_afrag(Mb, a0, k); __builtin_amdgcn_sched_barrier(0); }
        f32x4 av = {0.f, 0.f, 0.f, 0.f}, av2 = av;
#pragma unroll
        for (int k = 0; k < 10; ++k) {
            if (k + 3 < 10) af[k + 3] = rd_afrag(Mb, a0, k + 3);
            __builtin_amdgcn_sched_barrier(0);
            if (!VONE && (k & 1)) av2 = smf(af[k], Wv[k], av2, sidx); else av = smf(af[k], Wv[k], av, sidx);
            if (PCNT == 0 && k == 1 && s >= 1 && tid == 448) cnt_add(P.ws, C_CNTZM + (s - 1) * 128);
            __builtin_amdgcn_sched_barrier(0);
        }
        if (!VONE) av += av2;
        const float v0 = swap32_sum(av[0]), v1 = swap32_sum(av[1]), v2 = swap32_sum(av[2]), v3 = swap32_sum(av[3]);
        *(LAS v2u*)vtw = v2u{pk(v0, v1), pk(v2, v3)};
        if (VCAN == 0) st32s(ws, last ? OOB : cano, (unsigned)(s + 1));
        ts.mark(2, on);
        bar();
        if (VCAN == 1) st32s(ws, last ? OOB : cano, (unsigned)(s + 1));
        ts.mark(3, on);
        const v4u A0 = rd_afrag(VT, a0, 0);
        __builtin_amdgcn_sched_barrier(0);
        const v4u A1 = rd_afrag(VT, a0, 1);
        __builtin_amdgcn_sched_barrier(0);
        f32x4 az[5];
#pragma unroll
        for (int t = 0; t < 5; ++t) { az[t] = f32x4{0.f, 0.f, 0.f, 0.f}; az[t] = smf(A0, Wo[2 * t], az[t], sidx); }
        __builtin_amdgcn_sched_barrier(0);
#pragma unroll
        for (int t = 0; t < 5; ++t) az[t] = smf(A1, Wo[2 * t + 1], az[t], sidx);
        if (VCAN == 2) st32s(ws, last ? OOB : cano, (unsigned)(s + 1));
        if (SKEWST && w == 7 && s >= SK0 && s < SK0 + 8) skst(ws, lane, ((32 + h) * 8 + (s - SK0)) * 4 + 1);
        const unsigned a1 = safe(pk(fold2(az[0][0], az[1][0]), fold2(az[0][1], az[1][1]))), b1 = safe(pk(fold2(az[0][2], az[1][2]), fold2(az[0][3], az[1][3])));
        const unsigned a2 = safe(pk(fold2(az[2][0], az[3][0]), fold2(az[2][1], az[3][1]))), b2 = safe(pk(fold2(az[2][2], az[3][2]), fold2(az[2][3], az[3][3])));
        const auto ra = __builtin_amdgcn_permlane16_swap(a1, a2, false, false);
        const auto rb = __builtin_amdgcn_permlane16_swap(b1, b2, false, false);
        const v4u o4 = {ra[0], rb[0], ra[1], rb[1]};
        const v2u o1 = {safe(pk(swap32_sum(az[4][0]), swap32_sum(az[4][1]))), safe(pk(swap32_sum(az[4][2]), swap32_sum(az[4][3])))};
        ts.mark(4, on);
        {
            const unsigned zb = (s & 1) ? zb1 : zb0;
            st128so(ws, !last ? r4 : OOB, zb, o4);
            st64so(ws, !last ? r1 : OOB, zb, o1);
            if (DPERM) for (int z = 0; z < DPERM; ++z) __builtin_amdgcn_s_sleep(1);
            st128so(ws, q4, OFF_ZMP + (unsigned)s * (40u * 8192u), o4);
            st64so(ws, q1, OFF_ZMP + (unsigned)s * (40u * 8192u), o1);
        }
        if (PCNT == 1 && s >= 1 && tid == 0) cnt_add(P.ws, C_CNTZM + (s - 1) * 128);
        ts.mark(6, on);
    }
    drain(); bar();
    if (tid == 0) cnt_add(P.ws, C_CNTZM + (NSEG - 1) * 128);
    ts.dump(P, 32 + h, nfail);
}

__device__ __forceinline__ v2u rd2(lds_t p, int o0, int o1) { v2u r; asm volatile("ds_read2_b32 %0, %1 offset0:%2 offset1:%3" : "=v"(r) : "v"((unsigned)(unsigned long long)p), "i"(o0), "i"(o1) : "memory"); return r; }
__device__ __forceinline__ void role_red(const Params& P, lds_t L, int c, int cls, int np) {
    const int tid = threadIdx.x, lane = tid & 63, w = __builtin_amdgcn_readfirstlane(tid >> 6);
    const rsrc_t ws = mkrs(P.ws, WS_USED);
    lds_t ZB = L;
    lds_t ZR = L + 65536;
    volatile LAS int* zflag = (volatile LAS int*)(L + 65536 + 8192);
    const unsigned lo = lane * 16;
    if (tid == 0) { zflag[0] = 0; zflag[1] = 0; }
    if (w != 0) {
        const v4u wsd = mksrd(P.ws, WS_USED);
        if (TLB0 && w == 2) { dma16s(wsd, L + 81920, lo, OFF_ZX + c * 32768u); dma16s(wsd, L + 81920, lo, OFF_ZX + (40 + c) * 32768u); dma16s(wsd, L + 81920, lo, OFF_ZX + (80 + c) * 32768u); dma16s(wsd, L + 81920, lo, OFF_ZMP + c * 8192u); }
        if (TLB0 && w == 3) { dma16s(wsd, L + 81920, lo, OFF_KX + c * 8192u); dma16s(wsd, L + 81920, lo, OFF_QX + c * 8192u); dma16s(wsd, L + 81920, lo, OFF_PXX + c * 1024u); dma16s(wsd, L + 81920, lo, OFF_PXM + c * 1024u); }
        unsigned nfz = 0;
        for (int s = 0; s < NSEG - 1; ++s) {
            if (w >= 4) {
                if (!wait_cnt(ws, C_CNTZ + s * 128, 32, 0x300 + s)) return;
#pragma unroll
                for (int k = 0; k < 8; ++k) dma16s(wsd, ZB + (s & 1) * 32768 + (8 * (w - 4) + k) * 1024, lo, OFF_ZX + (unsigned)(s * 40 + c) * 32768u + (8 * (w - 4) + k) * 1024);
                drain();
            }
            bar();
            if (TLBW && s + 4 < NSEG) {
                if (w == 2) dma16s(wsd, L + 81920, lo, OFF_ZX + (unsigned)((s + 4) * 40 + c) * 32768u + 31 * 1024);
                if (w == 3) { dma16s(wsd, L + 81920, lo, OFF_KX + (unsigned)((s + 4) * 32 + 31) * 8192u); dma16s(wsd, L + 81920, lo, OFF_QX + (unsigned)((s + 4) * 32 + 31) * 8192u);
                              dma16s(wsd, L + 81920, lo, OFF_PXX + (unsigned)((s + 4) * 32 + 31) * 2048u); dma16s(wsd, L + 81920, lo, OFF_PXM + (unsigned)((s + 4) * 32) * 512u); }
            }
            if (w == 1) {
                const unsigned zbase = zm_ring(s & 1, c, np);
                bool dead = !canary_wait(ws, can_off(2, cls, np), s + 1, P.dz, 0x380 + s);
                if (SKEWST && s >= SK0 && s < SK0 + 8) skst(ws, lane, ((64 + c) * 8 + (s - SK0)) * 4 + 2);
                int redo = 0;
                unsigned spins = 0;
                while (!dead) {
#pragma unroll
                    for (int k = 0; k < 8; ++k) dma16s(wsd, ZR + k * 1024, lo, zbase + k * 1024);
                    drain();
                    if (!redo) zflag[0] = s + 1;
                    if (SKEWST && !redo && s >= SK0 && s < SK0 + 8) skst(ws, lane, ((64 + c) * 8 + (s - SK0)) * 4 + 3);
                    unsigned mz = 0;
#pragma unroll
                    for (int k = 0; k < 8; ++k) mz = umax4(mz, *(LAS v4u*)(ZR + k * 1024 + lo));
                    if (__all(mz != 0xFFFFFFFFu)) break;
                    redo = 1 << 16;
                    for (int z = 0; z < P.retry; ++z) __builtin_amdgcn_s_sleep(1);
                    dead = !spin_more(ws, spins, 0x400 + s);
                }
                if (dead) { zflag[0] = 1 << 20; zflag[1] = 1 << 20; return; }
                zflag[1] = (s + 1) | redo;
                nfz += redo ? 1 : 0;
                const v4u ones = mk_ones();
#pragma unroll
                for (int k = 0; k < 8; ++k) st128so(ws, lo + (k & 3) * 1024, zbase + (k >> 2) * 4096, ones);
                drain();
            }
        }
        if ((STAMPS || STAMPLITE) && w == 1 && lane == 0) ((unsigned*)(P.ws + OFF_DBG + 8 * MB))[((64 + c) * 8 + 1) * 16 + 14] = nfz;
        return;
    }
    const int sidx = ((lane >> 3) & 1) ? 0xEEEE : 0x4444;
    const unsigned mo = (unsigned)(lane >> 4) * (2 * 16384) + fragoff(c >> 2) + (c & 3) * 256 + ((lane & 15) ^ (4 * (c & 1))) * 16;
    const unsigned cano = (c == 0 && lane < 4) ? can_off(0, lane, np) : OOB;
    lds_t zr = ZR + (lane >> 5) * 512 + (lane & 15) * 16 + ((lane >> 4) & 1) * 8;
    const unsigned mb0 = m_base(0, 0, np);
    unsigned nfail = 0;
    if (RPRIO) __builtin_amdgcn_s_setprio(3);
    TS ts; ts.init();
    TS tr; tr.initR();
    int slot_new = 1, slot_old = 0;
    for (int s = 0; s < NSEG - 1; ++s) {
        const bool on = s >= 8 && s < 60;
        tr.markR(0, on);
        bar();
        if (SKEWST && s >= SK0 && s < SK0 + 8) skst(ws, lane, ((64 + c) * 8 + (s - SK0)) * 4 + 3 + (1 << 14));
        tr.markR(1, on);
        const int sl = s & 1;
        lds_t zb = ZB + sl * 32768 + lane * 16;
        const unsigned pxb = px_base(sl, cls, np), pmb = pm_base(sl, cls, np);
        const unsigned mnew = mb0 + slot_new * (8 * 2 * BLK), mold = mb0 + slot_old * (8 * 2 * BLK);
        auto attempt = [&](bool first, bool& ok) -> v4u {
            v4u B[32];
#pragma unroll
            for (int k = 0; k < 32; ++k) B[k] = *(LAS v4u*)(zb + k * 1024);
            if (first) ok = canary_wait(ws, can_off(1, cls, np), s + 1, P.dr, 0x480 + s);
            asm volatile("" : "+v"(B[0]), "+v"(B[31]));
            tr.markR(2, on && first);
            if (SKEWST && first && s >= SK0 && s < SK0 + 8) skst(ws, lane, ((64 + c) * 8 + (s - SK0)) * 4 + 0);
            v4u p[20];
#pragma unroll
            for (int f = 0; f < 16; ++f) { p[f] = ld128so(ws, lo + (f & 3) * 1024, pxb + (f >> 3) * 16384 + ((f >> 2) & 1) * 4096); __builtin_amdgcn_sched_barrier(0); }
#pragma unroll
            for (int f = 0; f < 4; ++f) { p[16 + f] = ld128so(ws, lo + f * 1024, pmb); __builtin_amdgcn_sched_barrier(0); }
            tr.markR(3, on && first);
            f32x4 acc0 = {0.f, 0.f, 0.f, 0.f}, acc1 = acc0;
            unsigned mpa = 0, mpb = 0;
            v2u z[4][4]; unsigned zvv;
#pragma unroll
            for (int f = 0; f < 16; ++f) {
                const v4u x = B[2 * f], y = B[2 * f + 1];
                if (!ONEACC && (f & 1)) acc1 = smf(p[f], v8u{x.x, x.y, x.z, x.w, y.x, y.y, y.z, y.w}, acc1, sidx); else acc0 = smf(p[f], v8u{x.x, x.y, x.z, x.w, y.x, y.y, y.z, y.w}, acc0, sidx);
                mpa = umax3(mpa, p[f].x, p[f].y); mpb = umax3(mpb, p[f].z, p[f].w); asm volatile("" : "+v"(mpa), "+v"(mpb));
                if (first && f == RCAN) st32s(ws, cano, (unsigned)(s + 1));
                if (RSTAMPS && f == 0) { asm volatile("" :: "v"(acc0)); tr.markR(4, on && first); }
                __builtin_amdgcn_sched_barrier(0);
                if (f == ZPOS) {
                    if (RSTAMPS) { asm volatile("" :: "v"(acc0), "v"(acc1)); tr.markR(5, on && first); }
                    asm volatile("" : "+v"(acc0), "+v"(acc1));
                    if (first) { while (zflag[0] < s + 1) {} }
                    tr.markR(6, on && first);
                }
                if (f >= ZPOS && f < ZPOS + 8) {
                    const int e = f - ZPOS, F = e >> 1, b = e & 1;
                    z[F][2 * b] = rd2(zr + F * 2048 + b * 1024, 0, 64); z[F][2 * b + 1] = rd2(zr + F * 2048 + b * 1024, 1, 65);
                    if (e == 7) asm volatile("ds_read_b32 %0, %1 offset:4" : "=v"(zvv) : "v"((unsigned)(unsigned long long)(lds_t)zflag) : "memory");
                    __builtin_amdgcn_sched_barrier(0);
                }
            }
            asm volatile("s_waitcnt lgkmcnt(0)" : "+v"(zvv), "+v"(z[0][0]), "+v"(z[0][1]), "+v"(z[0][2]), "+v"(z[0][3]), "+v"(z[1][0]), "+v"(z[1][1]), "+v"(z[1][2]), "+v"(z[1][3]),
                         "+v"(z[2][0]), "+v"(z[2][1]), "+v"(z[2][2]), "+v"(z[2][3]), "+v"(z[3][0]), "+v"(z[3][1]), "+v"(z[3][2]), "+v"(z[3][3]) :: "memory");
            __builtin_amdgcn_sched_barrier(0);
#pragma unroll
            for (int F = 0; F < 4; ++F) {
                const v8u bz = {z[F][0].x, z[F][0].y, z[F][1].x, z[F][1].y, z[F][2].x, z[F][2].y, z[F][3].x, z[F][3].y};
                if (!ONEACC && (F & 1)) acc1 = smf(p[16 + F], bz, acc1, sidx); else acc0 = smf(p[16 + F], bz, acc0, sidx);
                mpa = umax3(mpa, p[16 + F].x, p[16 + F].y); mpb = umax3(mpb, p[16 + F].z, p[16 + F].w); asm volatile("" : "+v"(mpa), "+v"(mpb));
                if (first && 16 + F == RCAN) st32s(ws, cano, (unsigned)(s + 1));
                __builtin_amdgcn_sched_barrier(0);
            }
            const unsigned mp = max(mpa, mpb);
            if (!ONEACC) acc0 += acc1;
            const unsigned d0 = safe(pk(swap32_sum(acc0[0]), swap32_sum(acc0[1]))), d1 = safe(pk(swap32_sum(acc0[2]), swap32_sum(acc0[3])));
            const auto r0 = __builtin_amdgcn_permlane16_swap(d0, d0, false, false);
            const auto r1 = __builtin_amdgcn_permlane16_swap(d1, d1, false, false);
            ok = ok && __all(mp != 0xFFFFFFFFu);
            int zz = __builtin_amdgcn_readfirstlane((int)zvv); while (zz < s + 1 || ((zz & 0xFFFF) < s + 1 && zz < (1 << 20))) zz = zflag[1];
            if (first) ok = ok && zz == s + 1;
            return v4u{r0[0], r1[0], r0[1], r1[1]};
        };
        bool ok = true;
        v4u o = attempt(true, ok);
        st128so(ws, ok ? mo : OOB, mnew, o);
        tr.markR(8, on);
        if (SKEWST && s >= SK0 && s < SK0 + 8) skst(ws, lane, ((64 + c) * 8 + (s - SK0)) * 4 + 1);
        if (!ok) {
            ++nfail;
            unsigned spins = 0;
            for (;;) {
                for (int z = 0; z < P.retry; ++z) __builtin_amdgcn_s_sleep(1);
                if (!spin_more(ws, spins, 0x500 + s)) return;
                ok = true;
                o = attempt(false, ok);
                if (ok) break;
            }
            st128so(ws, mo, mnew, o);
            st32s(ws, cano, (unsigned)(s + 1));
        }
        if (DEVDBG && (P.dbg & 1)) st128s(ws, lane < 16 ? OFF_DBG + (unsigned)(s + 1) * 10240u + (unsigned)c * 256 + lane * 16 : OOB, o);
        st128so(ws, s >= 1 ? mo : OOB, mold, mk_ones());
        slot_old = slot_new; slot_new = slot_new == 2 ? 0 : slot_new + 1;
    }
    ts.dump(P, 64 + c, nfail);
    tr.dumpR(P, c);
}

__device__ __forceinline__ void stage_x(v4u rx, lds_t dst, int s, const unsigned (&xo)[5], int w) {
#pragma unroll
    for (int m = 0; m < 5; ++m) dma16(rx, dst + (w + 8 * m) * 1024, xo[m] + (unsigned)s * (32u * 1280u));
}
__device__ __forceinline__ void init_xo(unsigned (&xo)[5], int w, int lane) {
#pragma unroll
    for (int m = 0; m < 5; ++m) {
        const int S = (w + 8 * m) * 64 + lane, tok = S / 80, sp = S % 80;
        const int src = (sp & ~15) | ((sp & 15) ^ (tok & 15));
        xo[m] = (unsigned)(tok * 1280 + src * 16);
    }
}
__device__ __forceinline__ void role_xqk(const Params& P, lds_t L, int h) {
    const int tid = threadIdx.x, lane = tid & 63, w = __builtin_amdgcn_readfirstlane(tid >> 6);
    const rsrc_t ws = mkrs(P.ws, WS_USED);
    const v4u rq = mksrd(P.Wq, DM * 4096 * 2), rk = mksrd(P.Wk, DM * 4096 * 2), rx = mksrd(P.x, SEQ * DM * 2);
    v4u W[2][20];
    if (TLB0 && w == 0) dma16s(mksrd(P.ws, WS_USED), L + 140 * 1024, lane * 16, ((h & 1) ? OFF_KX : OFF_QX) + h * 8192u);
    {
        auto stage = [&](int st) {
            lds_t b = L + (st & 1) * 65536;
            stage_rows256<1>(rq, b, 128 * st, h, w, lane); stage_rows256<1>(rq, b + 16384, 128 * st + 64, h, w, lane);
            stage_rows256<1>(rk, b + 32768, 128 * st, h, w, lane); stage_rows256<1>(rk, b + 49152, 128 * st + 64, h, w, lane);
        };
        auto ord = [](int i) { return REVX ? 4 - i : i; };
        auto stg = [&](int i) { lds_t b = L + (i & 1) * 65536; const int st = ord(i);
            stage_rows256<1>(rq, b, 128 * st, h, w, lane); stage_rows256<1>(rq, b + 16384, 128 * st + 64, h, w, lane);
            stage_rows256<1>(rk, b + 32768, 128 * st, h, w, lane); stage_rows256<1>(rk, b + 49152, 128 * st + 64, h, w, lane); };
        stg(0); stg(1);
#pragma unroll
        for (int i = 0; i < 5; ++i) {
            const int st = REVX ? 4 - i : i;
            if (i < 4) asm volatile("s_waitcnt vmcnt(8)" ::: "memory"); else drain();
            bar();
            lds_t b = L + (i & 1) * 65536 + (w >> 2) * 32768;
#pragma unroll
            for (int k = 0; k < 4; ++k)
#pragma unroll
                for (int mt = 0; mt < 2; ++mt) W[mt][4 * st + k] = rd_denseNat(b + (k >> 1) * 16384, 256, 32 * (k & 1), 2 * (w & 3) + mt, lane);
            bar();
            if (i + 2 < 5) stg(i + 2);
        }
    }
    unsigned xo[5]; init_xo(xo, w, lane);
    const int XT = 40960;
    stage_x(rx, L, 0, xo, w);
    unsigned bo[4];
#pragma unroll
    for (int k = 0; k < 4; ++k) bo[k] = (lane & 15) * 1280 + (((4 * k + (lane >> 4)) ^ (lane & 15)) * 16);
    const unsigned obase = (w < 4 ? OFF_QX : OFF_KX) + (w & 3) * 2048 + lane * 16;
    gstamp(P, 128 + h, 2);
    for (int s = 0; s < NSEG; ++s) {
        if (s >= 1) asm volatile("s_waitcnt vmcnt(2)" ::: "memory"); else drain();
        bar();
        if (s == 1) gstamp(P, 128 + h, 3);
        if (s + 1 < NSEG) stage_x(rx, L + ((s + 1) & 1) * XT, s + 1, xo, w);
        lds_t xb = L + (s & 1) * XT;
        f32x4 acc[2][2];
#pragma unroll
        for (int a = 0; a < 2; ++a)
#pragma unroll
            for (int b = 0; b < 2; ++b) acc[a][b] = f32x4{0.f, 0.f, 0.f, 0.f};
        v4u bf[20][2];
#pragma unroll
        for (int k = 0; k < 3; ++k)
#pragma unroll
            for (int t = 0; t < 2; ++t) bf[k][t] = *(LAS v4u*)(xb + t * 16 * 1280 + (k >> 2) * 256 + bo[k & 3]);
#pragma unroll
        for (int k = 0; k < 20; ++k) {
            if (k + 3 < 20) {
#pragma unroll
                for (int t = 0; t < 2; ++t) bf[k + 3][t] = *(LAS v4u*)(xb + t * 16 * 1280 + ((k + 3) >> 2) * 256 + bo[(k + 3) & 3]);
            }
            __builtin_amdgcn_sched_barrier(0);
#pragma unroll
            for (int t = 0; t < 2; ++t) { acc[0][t] = mf32(W[0][k], bf[k][t], acc[0][t]); acc[1][t] = mf32(W[1][k], bf[k][t], acc[1][t]); }
            __builtin_amdgcn_sched_barrier(0);
        }
        if (s >= 2) { if (s + 1 < NSEG) asm volatile("s_waitcnt vmcnt(5)" ::: "memory"); else drain(); asm volatile("s_barrier" ::: "memory"); if (tid == 0) cnt_add(P.ws, C_CNTX + (s - 1) * 128); }
#pragma unroll
        for (int t = 0; t < 2; ++t)
            st128s(ws, obase + (unsigned)(s * 32 + h) * 8192u + t * 1024, v4u{pk(acc[0][t][0], acc[0][t][1]), pk(acc[0][t][2], acc[0][t][3]), pk(acc[1][t][0], acc[1][t][1]), pk(acc[1][t][2], acc[1][t][3])});
        if (s == 0) { drain(); bar(); if (tid == 0) cnt_add(P.ws, C_CNTX); }
    }
    drain(); bar();
    if (tid == 0) cnt_add(P.ws, C_CNTX + (NSEG - 1) * 128);
}
__device__ __forceinline__ void role_xv(const Params& P, lds_t L, int h) {
    const int tid = threadIdx.x, lane = tid & 63, w = __builtin_amdgcn_readfirstlane(tid >> 6);
    const rsrc_t ws = mkrs(P.ws, WS_USED);
    const v4u rv = mksrd(P.Wv, DM * 4096 * 2), ro = mksrd(P.Wo, 4096 * DM * 2), rx = mksrd(P.x, SEQ * DM * 2);
    v4u Wv[20], Wo[5][4];
    if (TLB0 && w == 0) dma16s(mksrd(P.ws, WS_USED), L + 140 * 1024, lane * 16, OFF_ZX + ((h & 1) * 40 + h) * 32768u);
    {
        auto chunk = [](int i) { return REVX ? (i < 4 ? 5 + i : i - 4) : i; };
        auto fills = [&](int i) { return i < 9 ? (chunk(i) < 5 ? 4 : 5) : 0; };
        auto stage = [&](int i) { lds_t b = L + (i % 3) * 40960; const int c = chunk(i);
            if (c < 5) { stage_rows256<1>(rv, b, 128 * c, h, w, lane); stage_rows256<1>(rv, b + 16384, 128 * c + 64, h, w, lane); } else stage_rows1280h<2>(ro, b, 32 * (c - 5), h, w, lane); };
        stage(0); stage(1); stage(2);
#pragma unroll
        for (int i = 0; i < 9; ++i) {
            wait_fills(fills(i + 1) + fills(i + 2));
            bar();
            lds_t b = L + (i % 3) * 40960;
            const int c = REVX ? (i < 4 ? 5 + i : i - 4) : i;
            if (c < 5) {
#pragma unroll
                for (int k = 0; k < 4; ++k) Wv[4 * c + k] = rd_denseNat(b + (k >> 1) * 16384, 256, 32 * (k & 1), w, lane);
            } else {
#pragma unroll
                for (int t = 0; t < 5; ++t) Wo[t][c - 5] = rd_densePerm(b, 1280, 0, 5 * w + t, lane);
            }
            bar();
            if (i + 3 < 9) stage(i + 3);
        }
    }
    unsigned xo[5]; init_xo(xo, w, lane);
    const int XT = 40960;
    lds_t VB = L + 2 * XT;
    gstamp(P, 160 + h, 2);
    stage_x(rx, L, 0, xo, w);
    unsigned bo[4];
#pragma unroll
    for (int k = 0; k < 4; ++k) bo[k] = (lane & 15) * 1280 + (((4 * k + (lane >> 4)) ^ (lane & 15)) * 16);
    for (int s = 0; s < NSEG; ++s) {
        if (s >= 1) asm volatile("s_waitcnt vmcnt(5)" ::: "memory"); else drain();
        bar();
        if (s == 1) gstamp(P, 160 + h, 3);
        if (s + 1 < NSEG) stage_x(rx, L + ((s + 1) & 1) * XT, s + 1, xo, w);
        lds_t xb = L + (s & 1) * XT;
        f32x4 av[2] = {f32x4{0.f, 0.f, 0.f, 0.f}, f32x4{0.f, 0.f, 0.f, 0.f}};
        v4u bf[20][2];
#pragma unroll
        for (int k = 0; k < 4; ++k)
#pragma unroll
            for (int t = 0; t < 2; ++t) bf[k][t] = *(LAS v4u*)(xb + t * 16 * 1280 + (k >> 2) * 256 + bo[k & 3]);
#pragma unroll
        for (int k = 0; k < 20; ++k) {
            if (k + 4 < 20) {
#pragma unroll
                for (int t = 0; t < 2; ++t) bf[k + 4][t] = *(LAS v4u*)(xb + t * 16 * 1280 + ((k + 4) >> 2) * 256 + bo[(k + 4) & 3]);
            }
            __builtin_amdgcn_sched_barrier(0);
#pragma unroll
            for (int t = 0; t < 2; ++t) av[t] = mf32(Wv[k], bf[k][t], av[t]);
            __builtin_amdgcn_sched_barrier(0);
        }
#pragma unroll
        for (int t = 0; t < 2; ++t) *(LAS v2u*)(VB + (t * 4 + (w >> 1)) * 1024 + lane * 16 + 8 * (w & 1)) = v2u{pk(av[t][0], av[t][1]), pk(av[t][2], av[t][3])};
        if (s >= 2) { if (s + 1 < NSEG) asm volatile("s_waitcnt vmcnt(5)" ::: "memory"); else drain(); }
        bar();
        if (s >= 2 && tid == 0) cnt_add(P.ws, C_CNTZ + (s - 1) * 128);
        v4u va[2][4];
#pragma unroll
        for (int k = 0; k < 4; ++k)
#pragma unroll
            for (int t = 0; t < 2; ++t) va[t][k] = *(LAS v4u*)(VB + (t * 4 + k) * 1024 + lane * 16);
        f32x4 az[2][5];
#pragma unroll
        for (int t = 0; t < 2; ++t)
#pragma unroll
            for (int c = 0; c < 5; ++c) az[t][c] = f32x4{0.f, 0.f, 0.f, 0.f};
#pragma unroll
        for (int k = 0; k < 4; ++k)
#pragma unroll
            for (int t = 0; t < 2; ++t)
#pragma unroll
                for (int c = 0; c < 5; ++c) az[t][c] = mf32(va[t][k], Wo[c][k], az[t][c]);
#pragma unroll
        for (int c = 0; c < 5; ++c) {
            const auto r01 = __builtin_amdgcn_permlane16_swap(pk(az[0][c][0], az[0][c][1]), pk(az[1][c][0], az[1][c][1]), false, false);
            const auto r23 = __builtin_amdgcn_permlane16_swap(pk(az[0][c][2], az[0][c][3]), pk(az[1][c][2], az[1][c][3]), false, false);
            st128s(ws, OFF_ZX + (unsigned)(s * 40 + 5 * w + c) * 32768u + h * 1024 + lane * 16, v4u{r01[0], r01[1], r23[0], r23[1]});
        }
        if (s == 0) { drain(); bar(); if (tid == 0) cnt_add(P.ws, C_CNTZ); }
    }
    drain(); bar();
    if (tid == 0) cnt_add(P.ws, C_CNTZ + (NSEG - 1) * 128);
}

__device__ __forceinline__ float bf2f(unsigned short b) { return __builtin_bit_cast(float, (unsigned)b << 16); }
__device__ __forceinline__ float bfround(float f) { return asf(pk(f, 0.f) << 16); }
__device__ __forceinline__ f32x4 mff32(float a, float b, f32x4 c) { return __builtin_amdgcn_mfma_f32_16x16x4f32(a, b, c, 0, 0, 0); }

__device__ __forceinline__ bool final_job(const Params& P, rsrc_t ws, lds_t L, int s, int cg) {
    const int tid = threadIdx.x, lane = tid & 63, w = __builtin_amdgcn_readfirstlane(tid >> 6);
    const unsigned lo = lane * 16;
    v4u ax[4][2], am[2], bx[4][4], bm[4];
    if (!wait_cnt(ws, C_CNTZ + s * 128, 32, 0x7C0 + s)) return false;
#pragma unroll
    for (int hh = 0; hh < 4; ++hh)
#pragma unroll
        for (int c = 0; c < 4; ++c) bx[hh][c] = ld128so(ws, lo, OFF_ZX + (unsigned)(s * 40 + 4 * cg + c) * 32768u + (4 * w + hh) * 1024);
    __builtin_amdgcn_sched_barrier(0);
    {
        unsigned spins = 0;
        const unsigned off = (lane == 0 ? C_CNTP : C_CNTZM) + s * 128;
        for (;;) {
            asm volatile("" ::: "memory");
            const unsigned v = ld32s(ws, off);
            if (__all(v + 1u >= 32u)) break;
            __builtin_amdgcn_s_sleep(1);
            if (!spin_more(ws, spins, 0x700 + s)) return false;
        }
    }
#pragma unroll
    for (int hh = 0; hh < 4; ++hh)
#pragma unroll
        for (int t = 0; t < 2; ++t) ax[hh][t] = ld128so(ws, lo + t * 1024, OFF_PXX + (unsigned)(s * 32 + 4 * w + hh) * 2048u);
#pragma unroll
    for (int t = 0; t < 2; ++t) am[t] = ld128so(ws, (lane >> 4) * 512 + (16 * t + (lane & 15)) * 16, OFF_PXM + (unsigned)(s * 32 + 4 * w) * 512u);
#pragma unroll
    for (int c = 0; c < 4; ++c) bm[c] = ld128so(ws, lo, OFF_ZMP + (unsigned)(s * 40 + 4 * cg + c) * 8192u + 4 * w * 256);
    f32x4 acc[2][4];
#pragma unroll
    for (int t = 0; t < 2; ++t)
#pragma unroll
        for (int c = 0; c < 4; ++c) acc[t][c] = f32x4{0.f, 0.f, 0.f, 0.f};
#pragma unroll
    for (int hh = 0; hh < 4; ++hh)
#pragma unroll
        for (int t = 0; t < 2; ++t)
#pragma unroll
            for (int c = 0; c < 4; ++c) acc[t][c] = mf32(ax[hh][t], bx[hh][c], acc[t][c]);
#pragma unroll
    for (int t = 0; t < 2; ++t)
#pragma unroll
        for (int c = 0; c < 4; ++c) acc[t][c] = mf32(am[t], bm[c], acc[t][c]);
    bar();
#pragma unroll
    for (int t = 0; t < 2; ++t)
#pragma unroll
        for (int c = 0; c < 4; ++c) *(LAS f32x4*)(L + ((w * 2 + t) * 4 + c) * 1024 + lo) = acc[t][c];
    bar();
    f32x4 v = {0.f, 0.f, 0.f, 0.f};
#pragma unroll
    for (int ww = 0; ww < 8; ++ww) v += *(LAS f32x4*)(L + (ww * 8 + w) * 1024 + lo);
    lds_t OT = L + 65536;
    {
        const int tok = 16 * (w >> 2) + 4 * (lane >> 4), col = 16 * (w & 3) + (lane & 15);
#pragma unroll
        for (int r = 0; r < 4; ++r) *(LAS unsigned short*)(OT + (tok + r) * 144 + col * 2) = (unsigned short)(pk(v[r], 0.f) & 0xffffu);
    }
    bar();
    if (tid < 256) {
        const int row = tid >> 3, ch = tid & 7;
        const v4u o = *(LAS v4u*)(OT + row * 144 + ch * 16);
        *(v4u*)((char*)P.out + (size_t)(32 * s + row) * 2560 + 1280 + cg * 128 + ch * 16) = o;
    }
    return true;
}

__device__ __forceinline__ void ssm_job(const Params& P, lds_t L, int d0) {
    const int tid = threadIdx.x, lane = tid & 63, w = __builtin_amdgcn_readfirstlane(tid >> 6);
    constexpr int UPAD = 256, UST = (UPAD + 2048) * 2 + 32;
    lds_t UT = L;
    lds_t KD = L + 37376;
    lds_t PR = L + 58368;
    lds_t RV = L + 74752;
    lds_t TC = L + 79104;
    constexpr int TST = 4144;
    bar();
    for (int i = tid; i < 8 * (UPAD / 2); i += 512) *(LAS unsigned*)(UT + (i / (UPAD / 2)) * UST + (i % (UPAD / 2)) * 4) = 0u;
#pragma unroll
    for (int it = 0; it < 4; ++it) {
        const int l = tid + 512 * it;
        const v4u v = *(const v4u*)((const char*)P.u + (size_t)l * 1280 + d0 * 2);
        const unsigned e[4] = {v.x, v.y, v.z, v.w};
#pragma unroll
        for (int c = 0; c < 8; ++c) *(LAS unsigned short*)(UT + c * UST + (UPAD + l) * 2) = (unsigned short)((c & 1) ? e[c >> 1] >> 16 : e[c >> 1] & 0xffffu);
    }
    const int d = d0 + w;
    {
        const float dt = bfround(expf(bf2f(((const unsigned short*)P.logdt)[d])));
#pragma unroll
        for (int it = 0; it < 2; ++it) {
            const int n = lane + 64 * it, o = d * 128 + n;
            const float ar = dt * bf2f(((const unsigned short*)P.Are)[o]), ai = dt * bf2f(((const unsigned short*)P.Aim)[o]);
            const float br = dt * bf2f(((const unsigned short*)P.Bre)[o]), bi = dt * bf2f(((const unsigned short*)P.Bim)[o]);
            const float cr = bf2f(((const unsigned short*)P.Cre)[o]), ci = bf2f(((const unsigned short*)P.Cim)[o]);
            *(LAS f32x4*)(PR + (w * 128 + n) * 16) = f32x4{ar * 1.4426950408889634f, ai * 0.15915494309189535f, cr * br - ci * bi, cr * bi + ci * br};
        }
    }
    bar();
#pragma unroll 1
    for (int r = 0; r < 4; ++r) {
        const int step = 1 << r, base = r == 0 ? 0 : 128 << r;
        const float la = (float)(base + 16 * step * (lane & 15)), lb = (float)(step * (lane & 15));
        f32x4 a0 = {0.f, 0.f, 0.f, 0.f}, a1 = a0;
#pragma unroll 2
        for (int i = 0; i < 32; ++i) {
            const f32x4 q = *(LAS f32x4*)(PR + (w * 128 + 4 * i + (lane >> 4)) * 16);
            const float ea = __builtin_amdgcn_exp2f(q[0] * la), eb = __builtin_amdgcn_exp2f(q[0] * lb);
            const float pa = __builtin_amdgcn_fractf(q[1] * la), pb = __builtin_amdgcn_fractf(q[1] * lb);
            const float ca = ea * __builtin_amdgcn_cosf(pa), sa = ea * __builtin_amdgcn_sinf(pa);
            const float er = eb * __builtin_amdgcn_cosf(pb), ei = eb * __builtin_amdgcn_sinf(pb);
            const float fr = q[2] * ca - q[3] * sa, fi = q[2] * sa + q[3] * ca;
            a0 = mff32(fr, er, a0); a1 = mff32(-fi, ei, a1);
        }
        a0 += a1;
        const int kb = r == 0 ? 0 : 128 + 128 * r;
#pragma unroll
        for (int rr = 0; rr < 4; ++rr) {
            const int j = 16 * (4 * (lane >> 4) + rr) + (lane & 15);
            if (r == 0 || j < 128 || (r == 3 && j == 128)) *(LAS float*)(KD + (w * 648 + kb + j) * 4) = a0[rr];
        }
    }
    bar();
    const int tile = w < 4 ? w : 11 - w;
    const int b = lane & 15, g = lane >> 4, sg = (8 - (b & 7)) & 7;
    lds_t ap = TC + sg * TST + (2048 - 16 * (g >> 1) - (b + sg) + 8 * (g & 1)) * 2;
    const float dsk_unused = 0.f; (void)dsk_unused;
#pragma unroll 1
    for (int c = 0; c < 8; ++c) {
        for (int x = tid; x < 2080; x += 512) {
            const int m = 2048 - x;
            float kv = 0.f;
            if (m >= 0 && m < 2048) {
                const int lt = (int)bfround((float)m);
                const int ix = lt < 256 ? lt : lt < 512 ? 256 + ((lt - 256) >> 1) : lt < 1024 ? 384 + ((lt - 512) >> 2) : 512 + ((lt - 1024) >> 3);
                kv = *(LAS float*)(KD + (c * 648 + ix) * 4);
            }
            *(LAS unsigned short*)(RV + x * 2) = (unsigned short)(pk(kv, 0.f) & 0xffffu);
        }
        bar();
        if (tid < 258) {
            const v4u p0 = *(LAS v4u*)(RV + tid * 16), p1 = *(LAS v4u*)(RV + tid * 16 + 16);
            const unsigned e[8] = {p0.x, p0.y, p0.z, p0.w, p1.x, p1.y, p1.z, p1.w};
#pragma unroll
            for (int sgm = 0; sgm < 8; ++sgm) {
                v4u o;
#pragma unroll
                for (int k = 0; k < 4; ++k) o[k] = (sgm & 1) ? __builtin_amdgcn_alignbit(e[(sgm >> 1) + k + 1], e[(sgm >> 1) + k], 16) : e[(sgm >> 1) + k];
                *(LAS v4u*)(TC + sgm * TST + tid * 16) = o;
            }
        }
        bar();
        lds_t bp = UT + c * UST + (UPAD + 256 * tile + 16 * b - 16 * (g >> 1) + 8 * (g & 1)) * 2;
        f32x4 y0 = {0.f, 0.f, 0.f, 0.f}, y1 = y0;
        const int ne = 8 * tile + 8;
#pragma unroll 4
        for (int e = 0; e < ne; e += 2) {
            y0 = mf32(*(LAS v4u*)(ap - 64 * e), *(LAS v4u*)(bp - 64 * e), y0);
            y1 = mf32(*(LAS v4u*)(ap - 64 * e - 64), *(LAS v4u*)(bp - 64 * e - 64), y1);
        }
        y0 += y1;
        const int l0 = 16 * (16 * tile + b) + 4 * g;
        lds_t up = UT + c * UST + (UPAD + l0) * 2;
        const v2u uu = *(LAS v2u*)up;
        const float dsk = bf2f(((const unsigned short*)P.Dsk)[d0 + c]);
        const float u0 = asf(uu.x << 16), u1 = asf(uu.x & 0xffff0000u), u2 = asf(uu.y << 16), u3 = asf(uu.y & 0xffff0000u);
        const v2u o = {pk(bfround(y0[0]) + bfround(u0 * dsk), bfround(y0[1]) + bfround(u1 * dsk)), pk(bfround(y0[2]) + bfround(u2 * dsk), bfround(y0[3]) + bfround(u3 * dsk))};
        bar();
        *(LAS v2u*)up = o;
    }
    bar();
#pragma unroll
    for (int it = 0; it < 4; ++it) {
        const int l = tid + 512 * it;
        unsigned e[8];
#pragma unroll
        for (int c = 0; c < 8; ++c) e[c] = *(LAS unsigned short*)(UT + c * UST + (UPAD + l) * 2);
        *(v4u*)((char*)P.out + (size_t)l * 2560 + d0 * 2) = v4u{e[0] | (e[1] << 16), e[2] | (e[3] << 16), e[4] | (e[5] << 16), e[6] | (e[7] << 16)};
    }
}
constexpr int NG = 64;
__device__ __forceinline__ void role_g(const Params& P, lds_t L, int g) {
    if (P.rolemask & 32) ssm_job(P, L, 8 * g);
    gstamp(P, 192 + g, 2);
    if (P.rolemask & 64) {
        const rsrc_t ws = mkrs(P.ws, WS_USED);
        for (int j = g; j < NSEG * 10; j += NG) { if (!final_job(P, ws, L, j / 10, j % 10)) return; }
    }
}

__device__ __forceinline__ int claim_role(const Params& P, lds_t L) {
    if (threadIdx.x == 0) {
        const rsrc_t ws = mkrs(P.ws, WS_USED);
        int role = blockIdx.x;
        if (!(P.dbg & 8)) {
            const unsigned xcc = (unsigned)__builtin_amdgcn_s_getreg(20 | (3 << 11)) & 7u;
            const unsigned t = __hip_atomic_fetch_add((GAS unsigned*)(P.ws + C_TICK + xcc * 128), 1u, __ATOMIC_RELAXED, __HIP_MEMORY_SCOPE_AGENT) + 1u;
            asm volatile("s_waitcnt vmcnt(0)" ::: "memory");
            __hip_atomic_fetch_add((GAS unsigned*)(P.ws + C_ARR), 1u, __ATOMIC_RELAXED, __HIP_MEMORY_SCOPE_AGENT);
            if (t < 32u) role = (int)(xcc * 32 + t);
            else {
                role = -1;
                unsigned spins = 0;
                for (;;) { const unsigned a = ld32s(ws, C_ARR) + 1u; if (a >= 256u) break; __builtin_amdgcn_s_sleep(4); if (++spins > SPIN_CAP) { set_abort(ws, 0x10); break; } }
                if (spins <= SPIN_CAP) {
                    unsigned k = __hip_atomic_fetch_add((GAS unsigned*)(P.ws + C_OVF), 1u, __ATOMIC_RELAXED, __HIP_MEMORY_SCOPE_AGENT) + 1u;
                    for (int j = 0; j < 8; ++j) { const unsigned n = ld32s(ws, C_TICK + j * 128) + 1u, fr = n < 32u ? 32u - n : 0u; if (role < 0) { if (k < fr) role = j * 32 + (int)(n + k); else k -= fr; } }
                }
            }
        } else role = (blockIdx.x & 7) * 32 + (blockIdx.x >> 3);
        *(LAS int*)L = role;
    }
    __syncthreads();
    const int role = __builtin_amdgcn_readfirstlane(*(LAS int*)L);
    __syncthreads();
    return role;
}
__device__ __forceinline__ int near_parity(const Params& P, bool leader) {
    const rsrc_t ws = mkrs(P.ws, WS_USED);
    if (P.np >= 0) return P.np;
    if (leader && threadIdx.x < 64) {
        unsigned t[2];
#pragma unroll
        for (int par = 0; par < 2; ++par) {
            unsigned best = 0xFFFFFFFFu;
#pragma unroll
            for (int r = 0; r < 4; ++r) {
                unsigned long long c0, c1; unsigned v;
                asm volatile("s_memtime %0\n\ts_waitcnt lgkmcnt(0)" : "=s"(c0) :: "memory");
                v = ld32s(ws, C_PROBE + par * BLK + r * 1024); asm volatile("s_waitcnt vmcnt(0)" : "+v"(v) :: "memory");
                asm volatile("s_memtime %0\n\ts_waitcnt lgkmcnt(0)" : "=s"(c1) :: "memory");
                const unsigned d = (unsigned)(c1 - c0); if (r > 0 && d < best) best = d;
            }
            t[par] = best;
        }
        const unsigned a0 = ((C_PROBE >> 13) & 1);
        const unsigned np = t[0] <= t[1] ? a0 : (a0 ^ 1);
        if (threadIdx.x == 0) st32s(ws, C_NP, np);
    }
    unsigned spins = 0;
    for (;;) { asm volatile("" ::: "memory"); const unsigned v = __builtin_amdgcn_readfirstlane(ld32s(ws, C_NP)); if (v != 0xFFFFFFFFu) return (int)(v & 1); __builtin_amdgcn_s_sleep(2); if (!spin_more(ws, spins, 0x20)) return 0; }
}

__global__ void __launch_bounds__(512, 2) k_main(Params P) {
    extern __shared__ __attribute__((aligned(16))) char smem[];
    lds_t L = (lds_t)smem;
    const unsigned t_entry = STAMPLITE ? (unsigned)__builtin_amdgcn_s_memrealtime() : 0u;
    const int role = claim_role(P, L);
    if (role < 0) return;
    if (STAMPLITE && threadIdx.x == 0) ((unsigned*)(P.ws + OFF_DBG + 10 * MB))[role * 8 + 0] = t_entry;
    gstamp(P, role, 1);
    const int cls = role >> 5, r = role & 31;
    if (cls < 4) {
        if (r < 8) { if (P.rolemask & 1) role_qk(P, L, 8 * cls + r, cls, near_parity(P, role == 0)); }
        else if (r < 16) { if (P.rolemask & 2) role_vo(P, L, 8 * cls + r - 8, cls, near_parity(P, false)); }
        else if (r < 26) { if (P.rolemask & 4) role_red(P, L, 10 * cls + r - 16, cls, near_parity(P, false)); }
        else if (r < 30) { if (P.rolemask & 32) ssm_job(P, L, 8 * (64 + 4 * cls + r - 26)); }
    } else {
        if (r < 8) { if (P.rolemask & 8) role_xqk(P, L, 8 * (cls - 4) + r); }
        else if (r < 16) { if (P.rolemask & 16) role_xv(P, L, 8 * (cls - 4) + r - 8); }
        else role_g(P, L, 16 * (cls - 4) + r - 16);
    }
    gstamp(P, role, 4);
}

}

extern "C" void kernel_launch(void* const* d_in, const int* in_sizes, int n_in, void* d_out, int out_size, void* d_ws, size_t ws_size, hipStream_t stream) {
    char* base = (char*)(((uintptr_t)d_ws + (2u << 20) - 1) & ~(uintptr_t)((2u << 20) - 1));
    Params P;
    P.x = d_in[9]; P.mem0 = d_in[10]; P.Wq = d_in[11]; P.Wk = d_in[12]; P.Wv = d_in[13]; P.Wo = d_in[14];
    P.u = d_in[0]; P.Are = d_in[1]; P.Aim = d_in[2]; P.Bre = d_in[3]; P.Bim = d_in[4]; P.Cre = d_in[5]; P.Cim = d_in[6]; P.logdt = d_in[7]; P.Dsk = d_in[8];
    P.ws = base; P.out = d_out; P.np = -1; P.dbg = 0; P.cq = TUNE_CQ; P.dzx = TUNE_DZX; P.pad0 = 0; P.dq = TUNE_DQ; P.dr = TUNE_DR; P.dz = TUNE_DZ; P.retry = TUNE_RETRY; P.rolemask = 127;
    (void)hipFuncSetAttribute((const void*)k_main, hipFuncAttributeMaxDynamicSharedMemorySize, LDS_BYTES);
    (void)hipMemsetAsync(base, 0xFF, MEMSET_BYTES, stream);
    hipLaunchKernelGGL(k_main, dim3(256), dim3(512), LDS_BYTES, stream, P);
}
```
